```python
import torch

def _softmax(x, axis=-1):
    return torch.softmax(x, dim=axis)
def _gelu(x):
    return torch.nn.functional.gelu(x, approximate="tanh")
def _silu(x):
    return torch.nn.functional.silu(x)
def _sigmoid(x):
    return torch.sigmoid(x)
def _layernorm(x, axis=-1, eps=1e-5):
    m = x.mean(dim=axis, keepdim=True)
    v = x.var(dim=axis, keepdim=True, unbiased=False)
    return (x - m) / torch.sqrt(v + eps)
def _scatter_add(tgt, idx, src, axis=0):
    return tgt.clone().index_add_(axis, idx, src)
def _segment_sum(src, seg, n):
    out = torch.zeros((n,) + src.shape[1:], dtype=src.dtype)
    return out.index_add_(0, seg, src)
def _pad(x, pw):
    flat = tuple(v for pair in reversed(pw) for v in pair)
    return torch.nn.functional.pad(x, flat)

kernel_name = 'parallel_sum__spectral_gfnet__dense_activation_silu__prompt'

def setup_inputs():

    _w = (lambda a: (lambda t: t.to(torch.bfloat16) if t.is_floating_point() else t)(torch.as_tensor(a)))
    def _A_setup():
        B, S, D = 1, 2048, 512
        Df = D // 2 + 1
        return {
            "x": _w((lambda *s: torch.randn(*s, dtype=torch.bfloat16) * 0.05)(B, S, D)),
            "w_r": _w((lambda *s: torch.randn(*s, dtype=torch.bfloat16) * 0.8)(S, Df) * 0.02),
            "w_i": _w((lambda *s: torch.randn(*s, dtype=torch.bfloat16) * 0.8)(S, Df) * 0.02),
        }
    _A_in = _A_setup()

    def _B_setup():
        B, S, D, F = 1, 2048, 512, 1376
        return {
            "x": _w((lambda *s: torch.randn(*s, dtype=torch.bfloat16) * 0.05)(B, S, D)),
            "w1": _w((lambda *s: torch.randn(*s, dtype=torch.bfloat16) * 0.8)(D, F) * (D ** -0.5)),
            "w2": _w((lambda *s: torch.randn(*s, dtype=torch.bfloat16) * 0.1)(F, D) * (F ** -0.5)),
        }
    _B_in = _B_setup()

    return {
        "A_x": _A_in["x"],
        "A_w_r": _A_in["w_r"],
        "A_w_i": _A_in["w_i"],
        "B_x": _B_in["x"],
        "B_w1": _B_in["w1"],
        "B_w2": _B_in["w2"],
    }

def reference(A_x, A_w_r, A_w_i, B_x, B_w1, B_w2):
    def _A_ref(x, w_r, w_i):
        B, S, D = x.shape
        X = (lambda x, axes=(-2,-1): torch.fft.rfft2(x.float(), dim=axes))(x, axes=(-2, -1))
        W = w_r + 1j * w_i
        X = X * W
        y = (lambda x, s, axes=(-2,-1): torch.fft.irfft2(x, s=s, dim=axes).to(torch.bfloat16))(X, (S, D), axes=(-2, -1))
        return y

    def _B_ref(x, w1, w2):
        h = torch.einsum("bsd,df->bsf", x, w1)
        h = _silu(h)
        out = torch.einsum("bsf,fd->bsd", h, w2)
        return out

    x = A_x
    xn = _layernorm(x, axis=-1)
    return x + _A_ref(xn, A_w_r, A_w_i) + _B_ref(xn, B_w1, B_w2)
```

```cpp
#include <hip/hip_runtime.h>
#include <hip/hip_bf16.h>
#include <stdint.h>

#define S_ 2048
#define D_ 512
#define DF_ 257
#define F_ 1376

__device__ __forceinline__ float bf2f(uint16_t v) { return __uint_as_float(((uint32_t)v) << 16); }
__device__ __forceinline__ uint16_t f2bf(float f) {
    __hip_bfloat16 b = __float2bfloat16(f);
    return *reinterpret_cast<uint16_t*>(&b);
}
__device__ __forceinline__ float rbf(float f) { return bf2f(f2bf(f)); }

__global__ void k_ln(const uint16_t* x, float* xn) {
    int s = blockIdx.x; int t = threadIdx.x;
    float v[8]; float sum = 0.f;
    for (int j = 0; j < 8; j++) { v[j] = bf2f(x[s * D_ + t * 8 + j]); sum += v[j]; }
    for (int o = 32; o; o >>= 1) sum += __shfl_xor(sum, o);
    float mean = sum * (1.f / D_);
    float sq = 0.f;
    for (int j = 0; j < 8; j++) { float d = v[j] - mean; sq += d * d; }
    for (int o = 32; o; o >>= 1) sq += __shfl_xor(sq, o);
    float var = rbf(sq * (1.f / D_));
    float m = rbf(mean);
    float sd = rbf(sqrtf(rbf(var + 1e-5f)));
    for (int j = 0; j < 8; j++) xn[s * D_ + t * 8 + j] = rbf(rbf(v[j] - m) / sd);
}
__global__ void k_rowdft(const float* xn, float2* X) {
    int s = blockIdx.x; int k = threadIdx.x;
    __shared__ float row[D_]; __shared__ float2 tw[D_];
    for (int i = threadIdx.x; i < D_; i += blockDim.x) { row[i] = xn[s * D_ + i]; float sn, cs; sincospif(-2.f * i / D_, &sn, &cs); tw[i] = make_float2(cs, sn); }
    __syncthreads();
    if (k < DF_) {
        float re = 0, im = 0;
        for (int d = 0; d < D_; d++) { float2 w = tw[(d * k) & (D_ - 1)]; re += row[d] * w.x; im += row[d] * w.y; }
        X[(size_t)k * S_ + s] = make_float2(re, im);
    }
}
__global__ void k_col(const float2* X, const uint16_t* wr, const uint16_t* wi, float2* Y) {
    int k = blockIdx.x;
    __shared__ float2 a[S_]; __shared__ float2 b[S_]; __shared__ float2 tw[S_];
    for (int i = threadIdx.x; i < S_; i += blockDim.x) { a[i] = X[(size_t)k * S_ + i]; float sn, cs; sincospif(-2.f * i / S_, &sn, &cs); tw[i] = make_float2(cs, sn); }
    __syncthreads();
    for (int q = threadIdx.x; q < S_; q += blockDim.x) {
        float re = 0, im = 0;
        for (int s = 0; s < S_; s++) { float2 w = tw[(q * s) & (S_ - 1)]; float2 v = a[s]; re += v.x * w.x - v.y * w.y; im += v.x * w.y + v.y * w.x; }
        float r = bf2f(wr[q * DF_ + k]), i = bf2f(wi[q * DF_ + k]);
        b[q] = make_float2(re * r - im * i, re * i + im * r);
    }
    __syncthreads();
    for (int s = threadIdx.x; s < S_; s += blockDim.x) {
        float re = 0, im = 0;
        for (int q = 0; q < S_; q++) { float2 w = tw[(q * s) & (S_ - 1)]; float2 v = b[q]; re += v.x * w.x + v.y * w.y; im += -v.x * w.y + v.y * w.x; }
        Y[(size_t)s * DF_ + k] = make_float2(re * (1.f / S_), im * (1.f / S_));
    }
}
__global__ void k_h(const float* xn, const uint16_t* w1, float* h) {
    int s = blockIdx.x;
    __shared__ float row[D_];
    for (int i = threadIdx.x; i < D_; i += blockDim.x) row[i] = xn[s * D_ + i];
    __syncthreads();
    for (int f = threadIdx.x; f < F_; f += blockDim.x) {
        float acc = 0;
        for (int d = 0; d < D_; d++) acc += row[d] * bf2f(w1[d * F_ + f]);
        acc = rbf(acc);
        h[(size_t)s * F_ + f] = rbf(acc / (1.f + __expf(-acc)));
    }
}
__global__ void k_final(const uint16_t* x, const float2* Y, const float* h, const uint16_t* w2, uint16_t* out) {
    int s = blockIdx.x;
    __shared__ float hr[F_]; __shared__ float2 y[DF_]; __shared__ float2 tw[D_];
    for (int i = threadIdx.x; i < F_; i += blockDim.x) hr[i] = h[(size_t)s * F_ + i];
    for (int i = threadIdx.x; i < DF_; i += blockDim.x) y[i] = Y[(size_t)s * DF_ + i];
    for (int i = threadIdx.x; i < D_; i += blockDim.x) { float sn, cs; sincospif(2.f * i / D_, &sn, &cs); tw[i] = make_float2(cs, sn); }
    __syncthreads();
    int d = threadIdx.x;
    float acc = 0;
    for (int f = 0; f < F_; f++) acc += hr[f] * bf2f(w2[f * D_ + d]);
    float ya = 0;
    for (int k = 1; k < 256; k++) { float2 w = tw[(d * k) & (D_ - 1)]; ya += y[k].x * w.x - y[k].y * w.y; }
    ya = (2.f * ya + y[0].x + ((d & 1) ? -y[256].x : y[256].x)) * (1.f / D_);
    float r = rbf(bf2f(x[s * D_ + d]) + rbf(ya));
    out[s * D_ + d] = f2bf(r + rbf(acc));
}

extern "C" void kernel_launch(void* const* d_in, const int* in_sizes, int n_in,
                              void* d_out, int out_size, void* d_ws, size_t ws_size,
                              hipStream_t stream) {
    const uint16_t* x = (const uint16_t*)d_in[0];
    const uint16_t* wr = (const uint16_t*)d_in[1];
    const uint16_t* wi = (const uint16_t*)d_in[2];
    const uint16_t* w1 = (const uint16_t*)d_in[4];
    const uint16_t* w2 = (const uint16_t*)d_in[5];
    char* ws = (char*)d_ws;
    float* xn = (float*)ws;
    float2* X = (float2*)(ws + (8 << 20));
    float2* Y = (float2*)(ws + (16 << 20));
    float* h = (float*)(ws + (24 << 20));
    k_ln<<<S_, 64, 0, stream>>>(x, xn);
    k_rowdft<<<S_, 320, 0, stream>>>(xn, X);
    k_col<<<DF_, 1024, 0, stream>>>(X, wr, wi, Y);
    k_h<<<S_, 256, 0, stream>>>(xn, w1, h);
    k_final<<<S_, 512, 0, stream>>>(x, Y, h, w2, (uint16_t*)d_out);
}
```

```cpp
#include <hip/hip_runtime.h>
#include <hip/hip_bf16.h>
#include <stdint.h>

#define S_ 2048
#define D_ 512
#define DF_ 257
#define F_ 1376

__device__ __forceinline__ float bf2f(uint16_t v) { return __uint_as_float(((uint32_t)v) << 16); }
__device__ __forceinline__ uint16_t f2bf(float f) {
    __hip_bfloat16 b = __float2bfloat16(f);
    return *reinterpret_cast<uint16_t*>(&b);
}
__device__ __forceinline__ float rbf(float f) { return bf2f(f2bf(f)); }

__global__ void k_ln(const uint16_t* x, float* xn) {
    int s = blockIdx.x; int t = threadIdx.x;
    float v[8]; float sum = 0.f;
    for (int j = 0; j < 8; j++) { v[j] = bf2f(x[s * D_ + t * 8 + j]); sum += v[j]; }
    for (int o = 32; o; o >>= 1) sum += __shfl_xor(sum, o);
    float mean = sum * (1.f / D_);
    float sq = 0.f;
    for (int j = 0; j < 8; j++) { float d = v[j] - mean; sq += d * d; }
    for (int o = 32; o; o >>= 1) sq += __shfl_xor(sq, o);
    float var = rbf(sq * (1.f / D_));
    float m = rbf(mean);
    float sd = rbf(sqrtf(rbf(var + 1e-5f)));
    for (int j = 0; j < 8; j++) xn[s * D_ + t * 8 + j] = rbf(rbf(v[j] - m) / sd);
}
__global__ void k_rowdft(const float* xn, float2* X) {
    int s = blockIdx.x; int k = threadIdx.x;
    __shared__ float row[D_]; __shared__ float2 tw[D_];
    for (int i = threadIdx.x; i < D_; i += blockDim.x) { row[i] = xn[s * D_ + i]; float sn, cs; sincospif(-2.f * i / D_, &sn, &cs); tw[i] = make_float2(cs, sn); }
    __syncthreads();
    if (k < DF_) {
        float re = 0, im = 0;
        for (int d = 0; d < D_; d++) { float2 w = tw[(d * k) & (D_ - 1)]; re += row[d] * w.x; im += row[d] * w.y; }
        X[(size_t)k * S_ + s] = make_float2(re, im);
    }
}
__global__ void k_col(const float2* X, const uint16_t* wr, const uint16_t* wi, float2* Y) {
    int k = blockIdx.x;
    __shared__ float2 a[S_]; __shared__ float2 b[S_]; __shared__ float2 tw[S_];
    for (int i = threadIdx.x; i < S_; i += blockDim.x) { a[i] = X[(size_t)k * S_ + i]; float sn, cs; sincospif(-2.f * i / S_, &sn, &cs); tw[i] = make_float2(cs, sn); }
    __syncthreads();
    for (int q = threadIdx.x; q < S_; q += blockDim.x) {
        float re = 0, im = 0;
        for (int s = 0; s < S_; s++) { float2 w = tw[(q * s) & (S_ - 1)]; float2 v = a[s]; re += v.x * w.x - v.y * w.y; im += v.x * w.y + v.y * w.x; }
        float r = bf2f(wr[q * DF_ + k]), i = bf2f(wi[q * DF_ + k]);
        b[q] = make_float2(re * r - im * i, re * i + im * r);
    }
    __syncthreads();
    for (int s = threadIdx.x; s < S_; s += blockDim.x) {
        float re = 0, im = 0;
        for (int q = 0; q < S_; q++) { float2 w = tw[(q * s) & (S_ - 1)]; float2 v = b[q]; re += v.x * w.x + v.y * w.y; im += -v.x * w.y + v.y * w.x; }
        Y[(size_t)s * DF_ + k] = make_float2(re * (1.f / S_), im * (1.f / S_));
    }
}
__global__ void k_h(const float* xn, const uint16_t* w1, float* h) {
    int s = blockIdx.x;
    __shared__ float row[D_];
    for (int i = threadIdx.x; i < D_; i += blockDim.x) row[i] = xn[s * D_ + i];
    __syncthreads();
    for (int f = threadIdx.x; f < F_; f += blockDim.x) {
        float acc = 0;
        for (int d = 0; d < D_; d++) acc += row[d] * bf2f(w1[d * F_ + f]);
        acc = rbf(acc);
        h[(size_t)s * F_ + f] = rbf(acc / (1.f + __expf(-acc)));
    }
}
__global__ void k_final(const uint16_t* x, const float2* Y, const float* h, const uint16_t* w2, uint16_t* out) {
    int s = blockIdx.x;
    __shared__ float hr[F_]; __shared__ float2 y[DF_]; __shared__ float2 tw[D_];
    for (int i = threadIdx.x; i < F_; i += blockDim.x) hr[i] = h[(size_t)s * F_ + i];
    for (int i = threadIdx.x; i < DF_; i += blockDim.x) y[i] = Y[(size_t)s * DF_ + i];
    for (int i = threadIdx.x; i < D_; i += blockDim.x) { float sn, cs; sincospif(2.f * i / D_, &sn, &cs); tw[i] = make_float2(cs, sn); }
    __syncthreads();
    int d = threadIdx.x;
    float acc = 0;
    for (int f = 0; f < F_; f++) acc += hr[f] * bf2f(w2[f * D_ + d]);
    float ya = 0;
    for (int k = 1; k < 256; k++) { float2 w = tw[(d * k) & (D_ - 1)]; ya += y[k].x * w.x - y[k].y * w.y; }
    ya = (2.f * ya + y[0].x + ((d & 1) ? -y[256].x : y[256].x)) * (1.f / D_);
    float r = rbf(bf2f(x[s * D_ + d]) + rbf(ya));
    out[s * D_ + d] = f2bf(r + rbf(acc));
}


typedef unsigned v4u __attribute__((ext_vector_type(4)));
template<int NW, int DEPTH>
__global__ __launch_bounds__(NW*64) void k_probe(const v4u* __restrict__ A, const v4u* __restrict__ B, int n, unsigned* sink, unsigned* stamps) {
    unsigned long long t0 = __builtin_amdgcn_s_memrealtime();
    int b = blockIdx.x; int ncol = 8, swz = 4;
    int nrow = gridDim.x / ncol;
    int xcd = b & 7, idx = b >> 3;
    int xr = xcd % swz, xc = xcd / swz; int ngc = 8 / swz;
    int rpx = nrow / swz, cpx = ncol / ngc;
    int lr = idx / cpx, lc = idx % cpx;
    int rt = xr * rpx + lr, ct = xc * cpx + lc;
    int steps = n / NW;
    int lane = threadIdx.x & 63, w = threadIdx.x >> 6;
    const v4u* pa = A + (size_t)rt * n * 64 + w * 64 + lane;
    const v4u* pb = B + (size_t)ct * n * 64 + w * 64 + lane;
    v4u acc = {0,0,0,0};
    v4u bufa[DEPTH], bufb[DEPTH];
    int s = 0;
#pragma unroll
    for (int j = 0; j < DEPTH; j++) { bufa[j] = pa[(size_t)s * NW * 64]; bufb[j] = pb[(size_t)s * NW * 64]; s++; }
    for (int i = DEPTH; i < steps; i += DEPTH) {
#pragma unroll
        for (int j = 0; j < DEPTH; j++) {
            acc ^= bufa[j]; acc += bufb[j];
            bufa[j] = pa[(size_t)s * NW * 64]; bufb[j] = pb[(size_t)s * NW * 64]; s++;
            __builtin_amdgcn_sched_barrier(0);
        }
    }
#pragma unroll
    for (int j = 0; j < DEPTH; j++) { acc ^= bufa[j]; acc += bufb[j]; }
    sink[blockIdx.x * NW * 64 + threadIdx.x] = acc.x ^ acc.y ^ acc.z ^ acc.w;
    __syncthreads();
    if (threadIdx.x == 0) {
        unsigned long long t1 = __builtin_amdgcn_s_memrealtime();
        stamps[blockIdx.x] = (unsigned)(t1 - t0);
    }
}
__global__ void k_spin(const unsigned* stamps) {
    unsigned sum = 0;
    for (int i = 0; i < 256; i++) sum += stamps[i];
    unsigned long long target = (unsigned long long)sum * 1000ull / 256ull;
    if (target > 2000000ull) target = 2000000ull;
    unsigned long long t0 = __builtin_amdgcn_s_memrealtime();
    while (__builtin_amdgcn_s_memrealtime() - t0 < target) __builtin_amdgcn_s_sleep(32);
}

extern "C" void kernel_launch(void* const* d_in, const int* in_sizes, int n_in,
                              void* d_out, int out_size, void* d_ws, size_t ws_size,
                              hipStream_t stream) {
    const uint16_t* x = (const uint16_t*)d_in[0];
    const uint16_t* wr = (const uint16_t*)d_in[1];
    const uint16_t* wi = (const uint16_t*)d_in[2];
    const uint16_t* w1 = (const uint16_t*)d_in[4];
    const uint16_t* w2 = (const uint16_t*)d_in[5];
    char* ws = (char*)d_ws;
    float* xn = (float*)ws;
    float2* X = (float2*)(ws + (8 << 20));
    float2* Y = (float2*)(ws + (16 << 20));
    float* h = (float*)(ws + (24 << 20));
    k_probe<8,4><<<256, 512, 0, stream>>>((const v4u*)x, (const v4u*)w1, 64, (unsigned*)(ws + (64<<20)), (unsigned*)(ws + (65<<20)));
    k_spin<<<1, 1, 0, stream>>>((const unsigned*)(ws + (65<<20)));
    k_ln<<<S_, 64, 0, stream>>>(x, xn);
    k_rowdft<<<S_, 320, 0, stream>>>(xn, X);
    k_col<<<DF_, 1024, 0, stream>>>(X, wr, wi, Y);
    k_h<<<S_, 256, 0, stream>>>(xn, w1, h);
    k_final<<<S_, 512, 0, stream>>>(x, Y, h, w2, (uint16_t*)d_out);
}
```

```cpp
#include <hip/hip_runtime.h>
#include <stdint.h>

typedef unsigned short u16;
typedef unsigned int u32;
typedef unsigned long long u64;
typedef u32 u32x4 __attribute__((ext_vector_type(4)));
typedef u32 u32x2 __attribute__((ext_vector_type(2)));
typedef short s16x4 __attribute__((ext_vector_type(4)));
typedef float f32x2 __attribute__((ext_vector_type(2)));
typedef float f32x4 __attribute__((ext_vector_type(4)));
typedef float f32x16 __attribute__((ext_vector_type(16)));
typedef __bf16 bf16x8 __attribute__((ext_vector_type(8)));
typedef __bf16 bf16x2 __attribute__((ext_vector_type(2)));
typedef _Float16 f16x8 __attribute__((ext_vector_type(8)));
typedef _Float16 f16x2 __attribute__((ext_vector_type(2)));
#define LAS __attribute__((address_space(3)))
#define DI __device__ __forceinline__

#define STAMPS 0

constexpr u32 WS_EPOCH = 0;
constexpr u32 WS_FW = 4096, WS_FY = WS_FW + 16384;
constexpr u32 WS_FX = WS_FY + 16384, WS_FH = WS_FX + 8192;
constexpr u32 WS_X = 65536;
constexpr u32 WS_W = WS_X + (2u << 20);
constexpr u32 WS_Y = WS_W + 259u * 8192u;
constexpr u32 WS_H = WS_Y + (2u << 20);
constexpr u32 WS_END = WS_H + 32u * 43u * 4096u;
constexpr u32 WS_STAMP = 16u << 20;

constexpr u32 L_XS = 0;
constexpr u32 L_FR = 65536;
constexpr u32 L_SCR = L_FR;
constexpr u32 L_W2E = L_FR + 12288;
constexpr u32 L_W2X = L_FR + 28672;
constexpr u32 L_W1A = 98304;
constexpr u32 L_SM = 159744;
constexpr u32 L_ST = L_SM;
constexpr u32 L_CS = L_SM + 1024;
constexpr u32 L_CNT = L_SM + 2560;
constexpr u32 L_PB = L_CNT;
constexpr u32 L_PA = L_CNT + 16;
constexpr u32 L_XD = L_CNT + 32;
constexpr u32 L_FREE = L_CNT + 48;
constexpr u32 L_C1 = L_CNT + 64, L_CH = L_CNT + 68, L_CY = L_CNT + 72, L_MAXE = L_CNT + 76;
constexpr u32 L_CW = L_CNT + 100;
constexpr u32 L_EP = L_CNT + 80;
constexpr u32 L_G1 = L_CNT + 88, L_GH = L_CNT + 92, L_GY = L_CNT + 96;
constexpr u32 L_DUMMY = L_SM + 3072;
constexpr u32 L_TOTAL = 163840;

#if defined(__HIP_DEVICE_COMPILE__)
#pragma clang attribute push(__attribute__((target("no-packed-fp32-ops"))), apply_to = function)
#endif

struct Params {
    const u16* x; const u16* wr; const u16* wi; const u16* w1; const u16* w2; u16* out; unsigned char* ws;
};

DI f32x4 mfma16h(u32x4 a, u32x4 b, f32x4 c) { return __builtin_amdgcn_mfma_f32_16x16x32_f16(__builtin_bit_cast(f16x8, a), __builtin_bit_cast(f16x8, b), c, 0, 0, 0); }
DI f32x4 mfma16b(u32x4 a, u32x4 b, f32x4 c) { return __builtin_amdgcn_mfma_f32_16x16x32_bf16(__builtin_bit_cast(bf16x8, a), __builtin_bit_cast(bf16x8, b), c, 0, 0, 0); }
DI f32x16 mfma32b(u32x4 a, u32x4 b, f32x16 c) { return __builtin_amdgcn_mfma_f32_32x32x16_bf16(__builtin_bit_cast(bf16x8, a), __builtin_bit_cast(bf16x8, b), c, 0, 0, 0); }
DI u32 pkh(float a, float b) { f32x2 v = {a, b}; return __builtin_bit_cast(u32, __builtin_convertvector(v, f16x2)); }
DI u32 pkb(float a, float b) { f32x2 v = {a, b}; return __builtin_bit_cast(u32, __builtin_convertvector(v, bf16x2)); }
DI float blo(u32 v) { return __builtin_bit_cast(float, v << 16); }
DI float bhi(u32 v) { return __builtin_bit_cast(float, v & 0xffff0000u); }
DI float rbf(float f) { return blo(pkb(f, f)); }
DI float cosr(float rev) { return __builtin_amdgcn_cosf(rev); }
DI float sinr(float rev) { return __builtin_amdgcn_sinf(rev); }
DI float half_sum(float a) {
    u32 u = __builtin_bit_cast(u32, a);
    auto r = __builtin_amdgcn_permlane32_swap(u, u, false, false);
    return __builtin_bit_cast(float, (u32)r[0]) + __builtin_bit_cast(float, (u32)r[1]);
}
DI bool all0(u32 v) { return __builtin_amdgcn_ballot_w64(v != 0u) == 0ull; }
DI u32 umin(u32 a, u32 b) { return a < b ? a : b; }
DI u32 umax(u32 a, u32 b) { return a > b ? a : b; }
#define WAITV(n) asm volatile("s_waitcnt vmcnt(%0)" ::"i"(n) : "memory")
#define WAITL() asm volatile("s_waitcnt lgkmcnt(0)" ::: "memory")
#define BAR() asm volatile("s_waitcnt lgkmcnt(0)\n\ts_barrier" ::: "memory")
#define PIN(x) asm volatile("" : "+v"(x))
#define SB() __builtin_amdgcn_sched_barrier(0)

DI void glds4(u32 v0, u32 v1, u32 v2, u32 v3, const void* sb, u32 lds) {
    asm volatile("s_mov_b32 m0, %5\n\ts_nop 0\n\t"
                 "global_load_lds_dwordx4 %0, %4\n\t"
                 "global_load_lds_dwordx4 %1, %4 offset:1024\n\t"
                 "global_load_lds_dwordx4 %2, %4 offset:2048\n\t"
                 "global_load_lds_dwordx4 %3, %4 offset:3072"
                 ::"v"(v0), "v"(v1), "v"(v2), "v"(v3), "s"(sb), "s"(lds) : "memory");
}
DI void glds3(u32 v0, u32 v1, u32 v2, const void* sb, u32 lds) {
    asm volatile("s_mov_b32 m0, %4\n\ts_nop 0\n\t"
                 "global_load_lds_dwordx4 %0, %3\n\t"
                 "global_load_lds_dwordx4 %1, %3 offset:1024\n\t"
                 "global_load_lds_dwordx4 %2, %3 offset:2048"
                 ::"v"(v0), "v"(v1), "v"(v2), "s"(sb), "s"(lds) : "memory");
}

template <int OFF> DI void glds1(u32 v, const void* sb, u32 lds) {
    asm volatile("s_mov_b32 m0, %2\n\ts_nop 0\n\tglobal_load_lds_dwordx4 %0, %1 offset:%3" ::"v"(v), "s"(sb), "s"(lds), "i"(OFF) : "memory");
}

typedef __amdgpu_buffer_rsrc_t rsrc_t;
DI u32x4 ld128(rsrc_t rs, u32 voff, u32 soff) { return __builtin_amdgcn_raw_buffer_load_b128(rs, voff, soff, 16); }
DI u32x2 ld64(rsrc_t rs, u32 voff, u32 soff) { return __builtin_amdgcn_raw_buffer_load_b64(rs, voff, soff, 16); }
DI void st128(rsrc_t rs, u32x4 v, u32 voff, u32 soff) { __builtin_amdgcn_raw_buffer_store_b128(v, rs, voff, soff, 16); }
DI void st64(rsrc_t rs, u32x2 v, u32 voff, u32 soff) { __builtin_amdgcn_raw_buffer_store_b64(v, rs, voff, soff, 16); }

typedef LAS unsigned char* lds_t;
DI u32x4 lds128(lds_t sm, u32 off) { return *(LAS u32x4*)(sm + off); }
DI f32x4 ldsf4(lds_t sm, u32 off) { return *(LAS f32x4*)(sm + off); }
DI u32x2 ldstr(lds_t sm, u32 off) { return __builtin_bit_cast(u32x2, __builtin_amdgcn_ds_read_tr16_b64_v4i16((LAS s16x4*)(sm + off))); }
DI u32x4 cat(u32x2 a, u32x2 b) { u32x4 r = {a.x, a.y, b.x, b.y}; return r; }

constexpr u32 SPIN_CAP = 1u << 16;

DI void post(u32 addr, u32 val) {
    asm volatile("s_mov_b64 exec, 1\n\tds_write_b32 %0, %1\n\ts_mov_b64 exec, -1" ::"v"(addr), "v"(val) : "memory");
}
DI void wait4(u32 addr, u32 target) {
#pragma nounroll
    for (u32 sp = 0; sp < (SPIN_CAP << 4); ++sp) {
        u32x4 v;
        asm volatile("ds_read_b128 %0, %1\n\ts_waitcnt lgkmcnt(0)" : "=&v"(v) : "v"(addr) : "memory");
        const u32 m = umin(umin(v.x, v.y), umin(v.z, v.w));
        if ((u32)__builtin_amdgcn_readfirstlane(m) >= target) break;
    }
}
DI u32 wait1(u32 addr, bool nap) {
    u32 r = 0;
#pragma nounroll
    for (u32 sp = 0; sp < (SPIN_CAP << 4); ++sp) {
        u32 v;
        asm volatile("ds_read_b32 %0, %1\n\ts_waitcnt lgkmcnt(0)" : "=&v"(v) : "v"(addr) : "memory");
        r = __builtin_amdgcn_readfirstlane(v);
        if (r != 0u) break;
        if (nap) __builtin_amdgcn_s_sleep(1);
    }
    return r;
}
DI u32 arrive(u32 addr) {
    u32 old;
    asm volatile("s_mov_b64 exec, 1\n\tds_add_rtn_u32 %0, %1, %2\n\ts_waitcnt lgkmcnt(0)\n\ts_mov_b64 exec, -1" : "=&v"(old) : "v"(addr), "v"(1u) : "memory");
    return __builtin_amdgcn_readfirstlane(old);
}
DI void publish(rsrc_t rs, u32 base, int slot, int R, u32 lo, u32 hi, int lane) {
    u32x2 v = {lo, hi}; st64(rs, v, lane < R ? base + lane * 2048 : 0x40000000u, slot * 8);
}

#define STAMP(i) do {} while (0)
#define STAMPR(i) do {} while (0)
#define STAMP_OUT() do {} while (0)
#define PSLEEP 2
#define YSLEEP 4
#define PF_LINES 0

__global__ __launch_bounds__(512, 2) void fused_kernel(Params p) {
    __shared__ __attribute__((aligned(16))) unsigned char smem[L_TOTAL];
    lds_t sm = (lds_t)smem;
    const int tid = threadIdx.x, lane = tid & 63;
    const int wave = __builtin_amdgcn_readfirstlane(tid >> 6);
    const int b = blockIdx.x;
    const int xr = b & 3, xc = (b >> 2) & 1, idx = b >> 3;
    const int T = 4 * xr + (idx >> 3), ct = 8 * xc + (idx & 7);
    const int T2 = 8 * xr + (idx >> 2), nt = 4 * xc + (idx & 3);
    const int rot = ((idx & 7) + 2 * (idx >> 3)) & 7;
    const int blk0 = ct < 14 ? 3 * ct : 40;
    const int l31 = lane & 31, hh = lane >> 5, i16 = lane & 15, g = lane >> 4;
    if (PF_LINES > 0) {
        const u64 pc = __builtin_amdgcn_s_getpc() & ~(u64)255;
        if (wave == 7) {
            constexpr int PER = (PF_LINES + 31) / 32;
            u32 ln = idx * PER + (lane < PER ? lane : 0); ln = ln < PF_LINES ? ln : 0;
            asm volatile("s_mov_b32 m0, %2\n\ts_nop 0\n\tglobal_load_lds_dword %0, %1" ::"v"(ln * 128), "s"(pc), "s"(L_DUMMY) : "memory");
        }
    }
    STAMPR(29);
    STAMP(0);

    if (wave < 4) {
        const int w = wave;
        const u32 row0 = 256 * ((lane >> 3) & 3) + 16 * T + 4 * w + hh;
        const u32 xo_e = row0 * 1024 + (((lane & 7) ^ g) << 4);
        const u32 xo_o = (row0 + 1024) * 1024 + (((lane & 7) ^ (4 + g)) << 4) - 1024;
        const u32 xs = L_XS + w * 16384;
#pragma unroll
        for (int j = 0; j < 3; ++j) glds4(xo_e, xo_o, xo_e, xo_o, (const char*)p.x + ((j + rot) & 7) * 128, xs + j * 4096);
        STAMP(1);
        const rsrc_t rs = __builtin_amdgcn_make_buffer_rsrc(p.ws, 0, WS_END, 0x00020000);
        u32 wv0, wv1, wv2;
        { u32 q = lane, r = q / 12u; wv0 = r * 2752 + (q - r * 12u) * 16; q += 64; r = q / 12u; wv1 = r * 2752 + (q - r * 12u) * 16 - 1024; q += 64; r = q / 12u; wv2 = r * 2752 + (q - r * 12u) * 16 - 2048; }
        auto xpiece = [&](int j, int i) {
            const char* sb = (const char*)p.x + ((j + rot) & 7) * 128; const u32 slot = xs + (j & 3) * 4096;
            if (i == 0) glds1<0>(xo_e, sb, slot); else if (i == 1) glds1<1024>(xo_o, sb, slot); else if (i == 2) glds1<2048>(xo_e, sb, slot); else glds1<3072>(xo_o, sb, slot);
        };
        auto w1piece = [&](int j, int i) {
            const u32 slot = (j < 5 ? L_W1A + (4 * j + w) * 3072 : xs + (j - 5) * 3072);
            const char* sb = (const char*)p.w1 + (64 * ((j + rot) & 7) + 16 * w) * 2752 + blk0 * 64;
            if (i == 0) glds1<0>(wv0, sb, slot); else if (i == 1) glds1<1024>(wv1, sb, slot); else glds1<2048>(wv2, sb, slot);
        };
        u32 xrd[4], frd[4];
#pragma unroll
        for (int t = 0; t < 4; ++t) {
            const int tr = (t + w) & 3;
            xrd[t] = xs + l31 * 128 + (((2 * tr + hh) ^ ((l31 >> 1) & 7)) << 4);
            frd[t] = L_FR + tr * 1024 + lane * 16; PIN(frd[t]);
        }
        const u32 one = ((g ^ i16) & 1) ? 0u : 0x3f803f80u;
        u32x4 pones = {one, one, one, one}; PIN(pones);
        BAR();
        STAMP(2);
        u32x4 xf[32];
        f32x16 accd, accg;
        f32x4 accs = {0.f, 0.f, 0.f, 0.f};
#pragma unroll
        for (int r = 0; r < 16; ++r) { accd[r] = 0.f; accg[r] = 0.f; }
        {
            u32x4 ff[4];
            WAITV(8);
            STAMP(3);
#pragma unroll
            for (int t = 0; t < 4; ++t) xf[t] = lds128(sm, xrd[t]);
#pragma unroll
            for (int j = 0; j < 8; ++j) {
                SB();
#pragma unroll
                for (int t = 0; t < 4; ++t) {
                    accg = mfma32b(xf[4 * j + t], xf[4 * j + t], accg);
                    SB();
                    ff[t] = lds128(sm, frd[t] + j * 4096);
                    if (j < 5) xpiece(j + 3, t); else if (t < 3) w1piece(j - 5, t);
                    SB();
                    accs = mfma16b(pones, xf[4 * j + t], accs);
                    SB();
                }
                if (j < 7) {
                    if (j < 5) WAITV(8); else if (j == 5) WAITV(7); else WAITV(6);
                    if (j == 3) STAMP(4);
                    if (j == 6) STAMP(5);
#pragma unroll
                    for (int t = 0; t < 4; ++t) xf[4 * j + 4 + t] = lds128(sm, xrd[t] + ((j + 1) & 3) * 4096);
                }
                SB();
#pragma unroll
                for (int t = 0; t < 4; ++t) {
                    accd = mfma32b(xf[4 * j + t], ff[t], accd);
                    if (j == 7 && t < 3) { SB(); w1piece(3, t); SB(); }
                }
            }
        }
        STAMP(6);
        float rstd; u32 negm;
        {
            const bool b0 = lane & 1, b1 = lane & 2, b3 = lane & 8, b4 = lane & 16;
#define SEL2(c, t, f) ({ float t_ = (t), f_ = (f); PIN(t_); PIN(f_); (c) ? t_ : f_; })
#define SEL4(r) SEL2(b1, SEL2(b0, accg[(r) + 3], accg[(r) + 2]), SEL2(b0, accg[(r) + 1], accg[r]))
            float d1 = SEL2(b4, SEL2(b3, SEL4(12), SEL4(8)), SEL2(b3, SEL4(4), SEL4(0)));
            d1 = (((lane >> 2) & 1) == hh) ? d1 : 0.f;
            const float mean = ((lane & 16) ? accs[1] : accs[0]) * (1.f / 512.f);
            float var = half_sum(d1) * (1.f / 512.f) - mean * mean;
            var = var < 0.f ? 0.f : var;
            const u32 mb2 = pkb(mean, mean);
            const float mb = blo(mb2);
            const float sd = rbf(__builtin_amdgcn_sqrtf(rbf(rbf(var) + 1e-5f)));
            rstd = __builtin_amdgcn_rcpf(sd);
            negm = hh ? 0u : (mb2 ^ 0x80008000u);
            const u32 st = L_ST + w * 256;
            *(LAS float*)(sm + st + lane * 4) = hh ? mb * 512.f : rstd * (1.f / 32.f);
            if (ct == 0) {
                const float dc = l31 == 0 ? 1.f : 0.f;
#pragma unroll
                for (int q = 0; q < 4; ++q) {
                    const f32x4 m4 = ldsf4(sm, st + 128 + (8 * q + 4 * hh) * 4);
#pragma unroll
                    for (int e = 0; e < 4; ++e) accd[4 * q + e] -= dc * m4[e];
                }
            }
            float xv[16];
#pragma unroll
            for (int q = 0; q < 4; ++q) {
                const f32x4 r4 = ldsf4(sm, st + (8 * q + 4 * hh) * 4);
#pragma unroll
                for (int e = 0; e < 4; ++e) xv[4 * q + e] = accd[4 * q + e] * r4[e];
            }
            u32x4 a = {pkh(xv[0], xv[1]), pkh(xv[2], xv[3]), pkh(xv[4], xv[5]), pkh(xv[6], xv[7])};
            u32x4 c = {pkh(xv[8], xv[9]), pkh(xv[10], xv[11]), pkh(xv[12], xv[13]), pkh(xv[14], xv[15])};
            *(LAS u32x4*)(sm + xs + 9216 + lane * 16) = a;
            *(LAS u32x4*)(sm + xs + 10240 + lane * 16) = c;
            post(L_XD + w * 4, 1u);
            w1piece(4, 0); w1piece(4, 1); w1piece(4, 2);
        }
        STAMP(7);
        u32 w2v = (lane >> 3) * 1024 + (((lane & 7) ^ (g & 1)) << 4);
        auto w2slot = [&](int j) -> u32 {
            return j < 3 ? L_W1A + (u32)(j * 4 + w) * 4096
                 : j < 6 ? xs + (u32)(j - 3) * 4096
                 : j == 6 ? (w < 3 ? L_W1A + 49152 + (u32)w * 4096 : L_W2X)
                 : (j == 7 || j == 9) ? xs + 12288 : L_W2E + (u32)w * 4096;
        };
        const int r10 = (T2 & 7);
        auto w2kb = [&](int j) -> int { int q = j + r10; q = q >= 10 ? q - 10 : q; return j < 10 ? w + 4 * q : (w < 3 ? 40 + w : 42); };
        auto w2piece = [&](int j, int i) {
            glds1<0>(w2v, (const char*)p.w2 + w2kb(j) * 32768 + i * 8192 + nt * 128, w2slot(j) + i * 1024);
        };
        auto w2issue = [&](int j) { w2piece(j, 0); w2piece(j, 1); w2piece(j, 2); w2piece(j, 3); };
        f32x16 acc1[3];
        f32x4 accc[3];
#pragma unroll
        for (int n = 0; n < 3; ++n) {
            accc[n] = f32x4{0.f, 0.f, 0.f, 0.f};
#pragma unroll
            for (int r = 0; r < 16; ++r) acc1[n][r] = 0.f;
        }
        {
            const u32 w1l = (8 * hh + (i16 >> 2)) * 192 + (g & 1) * 32 + (lane & 3) * 8;
            u32 wa[4];
#pragma unroll
            for (int t = 0; t < 4; ++t) { wa[t] = w1l + L_W1A + ((t + w) & 3) * 3072; PIN(wa[t]); }
            u32x4 wf[3][3];
            auto w1read = [&](int s, int n) {
                const int j = s >> 2, t = s & 3;
                if (j == 5 && n == 0) { wa[t] = w1l + ((t + w) & 3) * 16384; PIN(wa[t]); }
                const u32 a = wa[t] + (j < 5 ? j * 12288 : (j - 5) * 3072) + n * 64;
                wf[s % 3][n] = cat(ldstr(sm, a), ldstr(sm, a + 768));
            };
            WAITV(9); post(L_PB + w * 4, 1u); wait4(L_PB, 1u);
            STAMP(8);
#pragma unroll
            for (int n = 0; n < 3; ++n) w1read(0, n);
#pragma unroll
            for (int n = 0; n < 3; ++n) w1read(1, n);
            u32x4 pv = {0u, 0u, 0u, 0u};
#pragma unroll
            for (int s = 0; s < 32; ++s) {
                const int k = s >> 3, u = s & 7;
                if (k < 3 && u == 4) {
                    if (k == 0) WAITV(7); else if (k == 1) WAITV(9); else WAITV(11);
                    post(L_PB + w * 4, k + 2);
                }
                if (k < 3 && u == 6) {
                    const u32 m = umin(umin(pv.x, pv.y), umin(pv.z, pv.w));
                    if (__builtin_expect((u32)__builtin_amdgcn_readfirstlane(m) < (u32)(k + 2), 0)) wait4(L_PB, k + 2);
                    asm volatile("" ::: "memory");
                }
#pragma unroll
                for (int n = 0; n < 3; ++n) {
                    SB();
                    acc1[n] = mfma32b(wf[s % 3][n], xf[s], acc1[n]);
                    SB();
                    if (s + 2 < 32) w1read(s + 2, n);
                    if (n == 0) {
                        if (s < 9) w1piece(5 + s / 3, s % 3);
                        else if (s < 17) w2piece(9 + ((s - 9) >> 2), (s - 9) & 3);
                        else if (s < 25) w2piece((s - 17) >> 2, (s - 17) & 3);
                        else if (s >= 28) w2piece(2, s - 28);
                    }
                    if (n == 0 && k < 3 && u == 5) pv = *(volatile LAS u32x4*)(sm + L_PB);
                }
                SB();
                if ((s & 3) == 0) {
#pragma unroll
                    for (int n = 0; n < 3; ++n) accc[n] = mfma16b(pones, wf[s % 3][n], accc[n]);
                }
                SB();
                if (s == 15) STAMP(9);
            }
        }
        STAMP(10);
        {
#pragma unroll
            for (int n = 0; n < 3; ++n) {
                const float c = (lane & 16) ? accc[n][1] : accc[n][0];
                const u32 h2 = pkb(c, c);
                const u32 pk = pkb(blo(h2), c - blo(h2));
                *(LAS u32*)(sm + L_CS + (n * 32 + l31) * 16 + w * 4) = pk;
            }
            post(L_PB + w * 4, 9u); wait4(L_PB, 9u);
            const u32x4 nm = {negm, negm, negm, negm};
#pragma unroll
            for (int n = 0; n < 3; ++n) acc1[n] = mfma32b(lds128(sm, L_CS + (n * 32 + l31) * 16), nm, acc1[n]);
        }
        wait1(L_PA + w * 4, false);
        const u32 e_lo = __builtin_amdgcn_readfirstlane(*(LAS u32*)(sm + L_EP)), e_hi = __builtin_amdgcn_readfirstlane(*(LAS u32*)(sm + L_EP + 4));
        STAMP(11);
        {
            const u32 ho = WS_H + (u32)((2 * T + (w >> 1)) * 43) * 4096 + (l31 & 3) * 1024 + hh * 256 +
                           (8 * (w & 1) + 4 * (l31 >> 4) + 2 * ((l31 >> 2) & 1) + ((l31 >> 3) & 1)) * 16;
            const float rs2 = rstd * -1.44269504089f;
#pragma unroll
            for (int n = 0; n < 3; ++n) {
                u32 pk[8];
#pragma unroll
                for (int q = 0; q < 8; ++q) {
                    if ((q & 1) == 0) { SB(); w2piece(3 + n, q >> 1); SB(); }
                    const float a0 = acc1[n][2 * q], a1 = acc1[n][2 * q + 1];
                    const float s0 = a0 * rstd * __builtin_amdgcn_rcpf(1.f + __builtin_amdgcn_exp2f(a0 * rs2));
                    const float s1 = a1 * rstd * __builtin_amdgcn_rcpf(1.f + __builtin_amdgcn_exp2f(a1 * rs2));
                    pk[q] = pkb(s0, s1);
                }
                const u32 hv = (ct < 14 || (ct == 14 && n == 2)) ? ho : 0x40000000u;
                u32x4 a = {pk[0], pk[1], pk[2], pk[3]}, c = {pk[4], pk[5], pk[6], pk[7]};
                st128(rs, a, hv, (u32)(blk0 + n) * 4096);
                st128(rs, c, hv + 512, (u32)(blk0 + n) * 4096);
                SB();
            }
        }
        STAMP(12);
        u32 rd0, rd1, rd0h, rd1h;
        {
            const u32 rrow = (16 * (g >> 1) + 4 * (g & 1) + (i16 >> 2)) * 128;
            const u32 sw = (i16 >> 3) & 1;
            rd0 = rrow + (((2 * (lane & 3)) ^ sw) << 4); rd1 = rrow + (((2 * (lane & 3) + 1) ^ sw) << 4);
            rd0h = rd0 + L_W1A + w * 4096; rd1h = rd1 + L_W1A + w * 4096;
            rd0 += xs; rd1 += xs;
            PIN(rd0); PIN(rd1); PIN(rd0h); PIN(rd1h);
        }
        auto w2frag = [&](int j, int cc) -> u32x4 {
            u32 a;
            if (j < 3) a = ((cc >> 1) ? rd1h : rd0h) + j * 16384;
            else if (j < 6) a = ((cc >> 1) ? rd1 : rd0) + (j - 3) * 4096;
            else if (j == 7 || j == 9) a = ((cc >> 1) ? rd1 : rd0) + 12288;
            else a = ((cc >> 1) ? rd1 : rd0) + (w2slot(j) - xs);
            a += (cc & 1) * 8;
            return cat(ldstr(sm, a), ldstr(sm, a + 1024));
        };
        WAITV(0);
        if (arrive(L_CH) == 3) publish(rs, WS_FH, T * 16 + ct, 4, e_lo, e_hi, lane);
        STAMP(13);
        u32x4 wreg[2][4];
#pragma unroll
        for (int q = 0; q < 2; ++q)
#pragma unroll
            for (int cc = 0; cc < 4; ++cc) wreg[q][cc] = w2frag(9 + q, cc);
        w2issue(6);
        WAITL();
        w2issue(7); w2issue(8);
        STAMP(20);
        {
            const u32 fo = WS_FH + (idx & 3) * 2048 + (T2 >> 1) * 128 + (i16 < 15 ? i16 : 14) * 8;
#pragma nounroll
            for (u32 sp = 0; sp < SPIN_CAP; ++sp) {
                const u32x2 f = ld64(rs, fo, 0);
                if (all0((f.x ^ e_lo) | (f.y ^ e_hi))) break;
                __builtin_amdgcn_s_sleep(PSLEEP);
            }
            asm volatile("" ::: "memory");
        }
        STAMP(14);
        f32x4 acc[4][4];
#pragma unroll
        for (int c = 0; c < 4; ++c)
#pragma unroll
            for (int cc = 0; cc < 4; ++cc) acc[c][cc] = f32x4{0.f, 0.f, 0.f, 0.f};
        const u32 hb = WS_H + (u32)(T2 * 43) * 4096;
        u32x4 hf[11][4];
        auto hload = [&](int j, int c) { hf[j][c] = ld128(rs, lane * 16, hb + w2kb(j) * 4096 + ((c + w) & 3) * 1024); };
        constexpr int HD = 4;
#pragma unroll
        for (int j = 0; j < HD; ++j)
#pragma unroll
            for (int c = 0; c < 4; ++c) hload(j, c);
        u32x4 wf[2][4];
#pragma unroll
        for (int cc = 0; cc < 4; ++cc) wf[0][cc] = w2frag(0, cc);
        SB();
#pragma unroll
        for (int j = 0; j < 10; ++j) {
#pragma unroll
            for (int c = 0; c < 4; ++c) {
#pragma unroll
                for (int cc = 0; cc < 4; ++cc) {
                    acc[c][cc] = mfma16b(j < 9 ? wf[j & 1][cc] : wreg[0][cc], hf[j][c], acc[c][cc]);
                    if (cc == 1 && j < 8) { SB(); wf[(j + 1) & 1][c] = w2frag(j + 1, c); SB(); }
                }
                SB();
                if (j + HD < 11) hload(j + HD, c);
                SB();
            }
            if (j == 2) post(L_FREE + w * 4, 1u);
            if (j == 0) STAMP(15);
            SB();
        }
#pragma unroll
        for (int c = 0; c < 4; ++c) PIN(hf[10][c]);
        if (w < 3) {
#pragma unroll
            for (int c = 0; c < 4; ++c)
#pragma unroll
                for (int cc = 0; cc < 4; ++cc) acc[c][cc] = mfma16b(wreg[1][cc], hf[10][c], acc[c][cc]);
        }
        STAMP(16);
#pragma unroll
        for (int k = 1; k < 4; ++k)
#pragma unroll
            for (int cc = 0; cc < 4; ++cc) *(LAS f32x4*)(sm + xs + (k - 1) * 4096 + cc * 1024 + lane * 16) = acc[k][cc];
        STAMP(17);
        BAR();
        STAMP(18);
        f32x4 o[4];
#pragma unroll
        for (int cc = 0; cc < 4; ++cc) o[cc] = acc[0][cc];
#pragma unroll
        for (int k = 1; k < 4; ++k) {
            const int src = (w - k) & 3;
#pragma unroll
            for (int cc = 0; cc < 4; ++cc) o[cc] += ldsf4(sm, L_XS + src * 16384 + (k - 1) * 4096 + cc * 1024 + lane * 16);
        }
        const u32x4 z0 = lds128(sm, L_SCR + w * 2048 + lane * 16), z1 = lds128(sm, L_SCR + w * 2048 + 1024 + lane * 16);
        u32x4 r0, r1;
#pragma unroll
        for (int cc = 0; cc < 4; ++cc) {
            const u32 za = cc < 2 ? z0[2 * cc] : z1[2 * cc - 4], zb = cc < 2 ? z0[2 * cc + 1] : z1[2 * cc - 3];
            const u32 oa = pkb(o[cc][0], o[cc][1]), ob = pkb(o[cc][2], o[cc][3]);
            const u32 ra = pkb(blo(za) + blo(oa), bhi(za) + bhi(oa)), rb = pkb(blo(zb) + blo(ob), bhi(zb) + bhi(ob));
            if (cc < 2) { r0[2 * cc] = ra; r0[2 * cc + 1] = rb; } else { r1[2 * cc - 4] = ra; r1[2 * cc - 3] = rb; }
        }
        const u32 srow = 256 * (4 * (i16 & 1) + w) + 8 * T2 + (i16 >> 1);
        u32x4* op = (u32x4*)(p.out + srow * 512 + 64 * nt + 16 * g);
        op[0] = r0; op[1] = r1;
        STAMP(19);
        STAMP_OUT();
    } else {
        const int v = wave - 4, ta = tid - 256;
        {
            const int bin = 16 * ct + (l31 & 15), ri = l31 >> 4;
            const bool nyq = (bin == 0 && ri == 1);
            const int ke = nyq ? 256 : bin;
            const int off = (ri && !nyq) ? 128 : 0;
            const float step = (float)ke * (1.f / 512.f);
            const float c16 = 2.f * cosr(step * 16.f);
            const int d0 = 64 * ((2 * v + rot) & 7) + 8 * hh;
            const float p0 = (float)((d0 * ke + off) & 511) * (1.f / 512.f), p1 = (float)(((d0 + 16) * ke + off) & 511) * (1.f / 512.f);
            float ca[8], cb[8];
#pragma unroll
            for (int j = 0; j < 8; ++j) { ca[j] = cosr(p0 + (float)j * step); cb[j] = cosr(p1 + (float)j * step); }
#pragma unroll
            for (int t = 0; t < 8; t += 2) {
                u32x4 f0 = {pkb(ca[0], ca[1]), pkb(ca[2], ca[3]), pkb(ca[4], ca[5]), pkb(ca[6], ca[7])};
                u32x4 f1 = {pkb(cb[0], cb[1]), pkb(cb[2], cb[3]), pkb(cb[4], cb[5]), pkb(cb[6], cb[7])};
                *(LAS u32x4*)(sm + L_FR + (8 * v + t) * 1024 + lane * 16) = f0;
                *(LAS u32x4*)(sm + L_FR + (8 * v + t + 1) * 1024 + lane * 16) = f1;
                if (t < 6) {
#pragma unroll
                    for (int j = 0; j < 8; ++j) { ca[j] = c16 * cb[j] - ca[j]; cb[j] = c16 * ca[j] - cb[j]; }
                }
            }
            if (ta < 32) *(LAS u32*)(sm + L_CNT + ta * 4) = 0u;
        }
        SB();
        STAMP(1);
        BAR();
        const rsrc_t rs = __builtin_amdgcn_make_buffer_rsrc(p.ws, 0, WS_END, 0x00020000);
        const rsrc_t rwr = __builtin_amdgcn_make_buffer_rsrc((void*)p.wr, 0, 2048 * 257 * 2, 0x00020000);
        const rsrc_t rwi = __builtin_amdgcn_make_buffer_rsrc((void*)p.wi, 0, 2048 * 257 * 2, 0x00020000);
        const u32x2 eld = ld64(rs, WS_EPOCH, 0);
        u32 wre[4], wim[4], wre2, wim2;
        const int ks0 = 2 * (b >> 6) + 8 * (b & 14) + 512 * ((b >> 4) & 3);
        const int wcol = 128 * (b & 1) + 32 * v + 2 * (lane >> 2);
        {
            const u32 vo = (8 * ((lane >> 1) & 1) + (lane & 1)) * 514 + wcol * 2;
#pragma unroll
            for (int r = 0; r < 4; ++r) {
                wre[r] = __builtin_amdgcn_raw_buffer_load_b32(rwr, vo, (ks0 + 128 * r) * 514, 0);
                wim[r] = __builtin_amdgcn_raw_buffer_load_b32(rwi, vo, (ks0 + 128 * r) * 514, 0);
            }
            const int e = lane & 7;
            const u32 eo = ((e >> 2) + 128 * (e & 3) + 8 * (b & 1)) * 514 + ((lane >> 3) == 1 ? 0 : 512);
            wre2 = __builtin_amdgcn_raw_buffer_load_b16(rwr, eo, ks0 * 514, 0);
            wim2 = __builtin_amdgcn_raw_buffer_load_b16(rwi, eo, ks0 * 514, 0);
        }
        SB();
        STAMP(2);
        u32x4 f8a0, f8a1, f8d0, f8d1, f16r, f16i, c1r[2], c1i[2], c4r[2], c4i[2];
        float t1c[2][4], t1s[2][4], t3c[2][4], t3s[2][4];
        {
            {
                const float ph = 0.25f * (float)((i16 & 1) - (g & 1)), st = (float)(i16 >> 1) * 0.125f;
                u32x4 f;
#pragma unroll
                for (int j = 0; j < 4; ++j) f[j] = pkh(0.25f * cosr(ph + st * (float)(2 * j)), 0.25f * cosr(ph + st * (float)(2 * j + 1)));
                const u32x4 z = {0u, 0u, 0u, 0u};
                f8a0 = g < 2 ? f : z; f8a1 = g < 2 ? z : f;
            }
            {
                const float ph = 0.25f * (float)((g & 1) - (i16 & 1));
                const int vr = (b >> 4) & 3, c0 = ((i16 >> 1) & 1) + vr;
                const float amp = ((g >> 1) == (i16 >> 3)) ? 0.5f : 0.f;
                const float st0 = (float)(4 * ((i16 >> 2) & 1) + (c0 & 3)) * 0.125f, st1 = (float)(4 * ((i16 >> 2) & 1) + ((c0 + 2) & 3)) * 0.125f;
#pragma unroll
                for (int j = 0; j < 4; ++j) {
                    f8d0[j] = pkh(amp * cosr(ph + st0 * (float)(2 * j)), amp * cosr(ph + st0 * (float)(2 * j + 1)));
                    f8d1[j] = pkh(amp * cosr(ph + st1 * (float)(2 * j)), amp * cosr(ph + st1 * (float)(2 * j + 1)));
                }
            }
            {
                float c[4], s[4];
#pragma unroll
                for (int j = 0; j < 4; ++j) { const float a = (float)(((4 * g + j) * i16) & 15) * (1.f / 16.f); c[j] = 0.25f * cosr(a); s[j] = 0.25f * sinr(a); }
                f16r = u32x4{pkh(c[0], c[1]), pkh(c[2], c[3]), pkh(s[0], s[1]), pkh(s[2], s[3])};
                f16i = u32x4{f16r.z ^ 0x80008000u, f16r.w ^ 0x80008000u, f16r.x, f16r.y};
            }
#pragma unroll
            for (int u = 0; u < 2; ++u) {
                const int ka = 2 * v + u;
                float c[4], s[4];
#pragma unroll
                for (int j = 0; j < 4; ++j) {
                    const float a2 = (float)((4 * g + j) * (8 * i16 + ka)) * (1.f / 2048.f);
                    c[j] = 0.25f * cosr(a2 * 16.f); s[j] = 0.25f * sinr(a2 * 16.f);
                    t1c[u][j] = cosr(a2); t1s[u][j] = sinr(a2);
                }
                c1r[u] = u32x4{pkh(c[0], c[1]), pkh(c[2], c[3]), pkh(s[0], s[1]), pkh(s[2], s[3])};
                c1i[u] = u32x4{c1r[u].z ^ 0x80008000u, c1r[u].w ^ 0x80008000u, c1r[u].x, c1r[u].y};
#pragma unroll
                for (int j = 0; j < 4; ++j) {
                    const float a2 = (float)(i16 * (8 * (4 * g + j) + ka)) * (1.f / 2048.f);
                    c[j] = 0.25f * cosr(a2 * 16.f); s[j] = 0.25f * sinr(a2 * 16.f);
                    t3c[u][j] = cosr(a2); t3s[u][j] = sinr(a2);
                }
                c4i[u] = u32x4{pkh(s[0], s[1]), pkh(s[2], s[3]), pkh(c[0], c[1]), pkh(c[2], c[3])};
                c4r[u] = u32x4{c4i[u].z, c4i[u].w, c4i[u].x ^ 0x80008000u, c4i[u].y ^ 0x80008000u};
                PIN(c1r[u]); PIN(c1i[u]); PIN(c4r[u]); PIN(c4i[u]);
#pragma unroll
                for (int j = 0; j < 4; ++j) { PIN(t1c[u][j]); PIN(t1s[u][j]); PIN(t3c[u][j]); PIN(t3s[u][j]); }
            }
            PIN(f8a0); PIN(f8a1); PIN(f8d0); PIN(f8d1); PIN(f16r); PIN(f16i);
        }
        SB();
        STAMP(3);
        u32 e_lo, e_hi;
        {
            const u64 e = (((u64)eld.y << 32) | eld.x) + 1ull;
            e_lo = __builtin_amdgcn_readfirstlane((u32)e); e_hi = __builtin_amdgcn_readfirstlane((u32)(e >> 32));
            if (lane == 0) *(LAS u32x2*)(sm + L_EP) = u32x2{e_lo, e_hi};
            u32x4 a, c;
            const u32 w256 = wre2 | (wim2 << 16);
            typedef unsigned short us2 __attribute__((ext_vector_type(2)));
            us2 mx = __builtin_bit_cast(us2, w256 & 0x7f807f80u);
#pragma unroll
            for (int r = 0; r < 4; ++r) {
                a[r] = __builtin_amdgcn_perm(wim[r], wre[r], 0x05040100u);
                c[r] = __builtin_amdgcn_perm(wim[r], wre[r], 0x07060302u);
                mx = __builtin_elementwise_max(mx, __builtin_bit_cast(us2, wre[r] & 0x7f807f80u));
                mx = __builtin_elementwise_max(mx, __builtin_bit_cast(us2, wim[r] & 0x7f807f80u));
            }
            st128(rs, a, WS_W + wcol * 8192 + (lane & 3) * 16, (b & ~1) * 32);
            st128(rs, c, WS_W + wcol * 8192 + 8192 + (lane & 3) * 16, (b & ~1) * 32);
            if (ta < 24) {
                const int e2 = lane & 7, ks = ks0 + 8 * (b & 1) + (e2 >> 2) + 128 * (e2 & 3);
                const int km = lane < 8 ? ks : ((2048 - ks) & 2047);
                const u32 mo = (u32)(((km & 7) >> 1) * 64 + (km >> 9) * 16 + ((km >> 3) & 15)) * 32 + (km & 1) * 16 + ((km >> 7) & 3) * 4;
                __builtin_amdgcn_raw_buffer_store_b32(w256, rs, WS_W + (256 + (lane >> 3)) * 8192 + mo, 0, 16);
            }
            const u32 m1 = (u32)(mx.x > mx.y ? mx.x : mx.y) >> 7;
            asm volatile("ds_max_u32 %0, %1" ::"v"(L_MAXE), "v"(m1) : "memory");
        }
        STAMP(4);
        WAITV(0);
        if (arrive(L_CW) == 3) {
            const u32 mx = *(LAS u32*)(sm + L_MAXE);
            publish(rs, WS_FW, b, 8, (e_lo << 8) | mx, (e_hi << 8) | (e_lo >> 24), lane);
        }
        STAMP(5);
        {
            wait1(L_XD + v * 4, true);
            STAMP(6);
            const u32x4 a = lds128(sm, L_XS + v * 16384 + 9216 + lane * 16), c = lds128(sm, L_XS + v * 16384 + 10240 + lane * 16);
            const u32 xo = WS_X + (16 * ct + (l31 & 15)) * 8192 + (16 * T + 4 * v + hh) * 32 + (l31 >> 4) * 16;
            st128(rs, a, xo, 0);
            st128(rs, c, xo + 64, 0);
            post(L_PA + v * 4, 1u);
            WAITV(0);
            STAMP(7);
            if (arrive(L_C1) == 3) publish(rs, WS_FX, ct * 16 + T, 4, e_lo, e_hi, lane);
        }
        u32 wexp;
        u32x4 wq[2], wn[2], wm[2], wk[2];
        float pr[2][4], pi[2][4], wsc;
        {
            const u32 t_lo = e_lo << 8, t_hi = (e_hi << 8) | (e_lo >> 24);
            const u32 fo = WS_FW + (b & 7) * 2048 + ta * 8;
            u32 mx = 0;
#pragma nounroll
            for (u32 sp = 0; sp < SPIN_CAP; ++sp) {
                const u32x2 f = ld64(rs, fo, 0);
                mx = f.x ^ t_lo;
                if (all0((mx >> 8) | (f.y ^ t_hi))) break;
                __builtin_amdgcn_s_sleep(2);
            }
            asm volatile("ds_max_u32 %0, %1" ::"v"(L_GH), "v"(mx & 255u) : "memory");
            wq[0] = ld128(rs, WS_W + ta * 32, b * 8192); wq[1] = ld128(rs, WS_W + ta * 32 + 16, b * 8192);
            post(L_PA + v * 4, 2u);
            wait4(L_PA, 2u);
            if (b == 0) {
#pragma unroll
                for (int u = 0; u < 2; ++u) {
                    wn[u] = ld128(rs, WS_W + ta * 32 + u * 16, 256 * 8192);
                    wm[u] = ld128(rs, WS_W + ta * 32 + u * 16, 257 * 8192);
                    wk[u] = ld128(rs, WS_W + ta * 32 + u * 16, 258 * 8192);
                }
            }
            wexp = __builtin_amdgcn_readfirstlane(*(LAS u32*)(sm + L_GH));
            wexp = wexp < 1u ? 1u : (wexp > 253u ? 253u : wexp);
            wsc = __builtin_bit_cast(float, (254u - wexp) << 23);
#pragma unroll
            for (int u = 0; u < 2; ++u)
#pragma unroll
                for (int r = 0; r < 4; ++r) { pr[u][r] = blo(wq[u][r]) * wsc; pi[u][r] = bhi(wq[u][r]) * wsc; PIN(pr[u][r]); PIN(pi[u][r]); }
            const u32 fx = WS_FX + (b & 3) * 2048 + (b >> 4) * 128 + i16 * 8;
#pragma nounroll
            for (u32 sp = 0; sp < SPIN_CAP; ++sp) {
                const u32x2 f = ld64(rs, fx, 0);
                if (all0((f.x ^ e_lo) | (f.y ^ e_hi))) break;
                __builtin_amdgcn_s_sleep(2);
            }
            asm volatile("" ::: "memory");
        }
        STAMP(8);
        {
            const int kd = b;
            const u32 xo = WS_X + i16 * 512 + v * 128 + g * 16;
            const u32x4 x0 = ld128(rs, xo, kd * 8192), x1 = ld128(rs, xo + 64, kd * 8192);
            SB();
            const f32x4 zero = {0.f, 0.f, 0.f, 0.f};
            {
                const u32 wa = L_SCR + (i16 >> 1) * 1040 + (g * 16 + 4 * v) * 16 + (i16 & 1) * 8;
#pragma unroll
                for (int e = 0; e < 4; ++e) {
                    const f32x4 t = mfma16h(e < 2 ? x0 : x1, (e & 1) ? f8a1 : f8a0, zero);
                    *(LAS u32x2*)(sm + wa + e * 16) = u32x2{pkh(t[0], t[1]), pkh(t[2], t[3])};
                }
            }
            post(L_PA + v * 4, 3u);
            STAMP(9);
            wait4(L_PA, 3u);
            STAMP(10);
            auto fwd = [&](int u, u32x4 d, f32x4& zr, f32x4& zi) {
                const f32x4 ur = mfma16h(d, c1r[u], zero), ui = mfma16h(d, c1i[u], zero);
                float a[4], c[4];
#pragma unroll
                for (int r = 0; r < 4; ++r) { a[r] = ur[r] * t1c[u][r] + ui[r] * t1s[u][r]; c[r] = ui[r] * t1c[u][r] - ur[r] * t1s[u][r]; }
                const u32x4 d2 = {pkh(a[0], a[1]), pkh(a[2], a[3]), pkh(c[0], c[1]), pkh(c[2], c[3])};
                zr = mfma16h(f16r, d2, zero); zi = mfma16h(f16i, d2, zero);
            };
            float yr[2][4], yi[2][4];
            if (kd == 0) {
#pragma unroll
                for (int u = 0; u < 2; ++u) {
                    u32x4 d = lds128(sm, L_SCR + ((8 - 2 * v - u) & 7) * 1040 + lane * 16);
                    d.z ^= 0x80008000u; d.w ^= 0x80008000u;
                    f32x4 zr, zi; fwd(u, d, zr, zi);
#pragma unroll
                    for (int r = 0; r < 4; ++r) {
                        const float h0r = 0.25f * (pr[u][r] + blo(wm[u][r]) * wsc), h0i = 0.25f * (pi[u][r] - bhi(wm[u][r]) * wsc);
                        const float h1r = 0.25f * wsc * (blo(wn[u][r]) + blo(wk[u][r])), h1i = 0.25f * wsc * (bhi(wn[u][r]) - bhi(wk[u][r]));
                        pr[u][r] = h0r + h1r; pi[u][r] = h0i + h1i;
                        const float qr = h0r - h1r, qi = h0i - h1i;
                        yr[u][r] = zr[r] * qr - zi[r] * qi; yi[u][r] = zr[r] * qi + zi[r] * qr;
                    }
                }
            } else {
#pragma unroll
                for (int u = 0; u < 2; ++u)
#pragma unroll
                    for (int r = 0; r < 4; ++r) { yr[u][r] = 0.f; yi[u][r] = 0.f; }
            }
            {
                const u32x4 d0 = lds128(sm, L_SCR + (2 * v) * 1040 + lane * 16), d1 = lds128(sm, L_SCR + (2 * v + 1) * 1040 + lane * 16);
                f32x4 zr[2], zi[2];
                fwd(0, d0, zr[0], zi[0]); fwd(1, d1, zr[1], zi[1]);
                post(L_PA + v * 4, 4u);
#pragma unroll
                for (int u = 0; u < 2; ++u)
#pragma unroll
                    for (int r = 0; r < 4; ++r) {
                        yr[u][r] += zr[u][r] * pr[u][r] - zi[u][r] * pi[u][r]; yi[u][r] += zr[u][r] * pi[u][r] + zi[u][r] * pr[u][r];
                    }
            }
            f32x4 qre[2], qim[2];
#pragma unroll
            for (int u = 0; u < 2; ++u) {
                const u32x4 d3 = {pkh(yi[u][0], yi[u][1]), pkh(yi[u][2], yi[u][3]), pkh(yr[u][0], yr[u][1]), pkh(yr[u][2], yr[u][3])};
                const f32x4 vi = mfma16h(d3, f16r, zero), vr = mfma16h(d3, f16i, zero);
                float a[4], c[4];
#pragma unroll
                for (int r = 0; r < 4; ++r) { a[r] = vr[r] * t3c[u][r] - vi[r] * t3s[u][r]; c[r] = vi[r] * t3c[u][r] + vr[r] * t3s[u][r]; }
                const u32x4 d4 = {pkh(a[0], a[1]), pkh(a[2], a[3]), pkh(c[0], c[1]), pkh(c[2], c[3])};
                qre[u] = mfma16h(c4r[u], d4, zero); qim[u] = mfma16h(c4i[u], d4, zero);
            }
            wait4(L_PA, 4u);
#pragma unroll
            for (int r = 0; r < 4; ++r) {
                const u32 a = L_SCR + (16 * (4 * g + r) + i16) * 16 + v * 4;
                *(LAS u32*)(sm + a) = pkh(qre[0][r], qre[1][r]);
                *(LAS u32*)(sm + a + 4112) = pkh(qim[0][r], qim[1][r]);
            }
            post(L_PA + v * 4, 5u);
            STAMP(11);
            wait4(L_PA, 5u);
#pragma unroll
            for (int f = 0; f < 2; ++f) {
                const u32x4 d = lds128(sm, L_SCR + (g & 1) * 4112 + (64 * v + 32 * f + 2 * i16 + (g >> 1)) * 16);
                const f32x4 a = mfma16h(f8d0, d, zero), c = mfma16h(f8d1, d, zero);
                const u32x4 o = {pkh(a[0], a[1]), pkh(a[2], a[3]), pkh(c[0], c[1]), pkh(c[2], c[3])};
                st128(rs, o, WS_Y + (64 * v + 32 * f) * 32 + i16 * 64 + g * 16, kd * 8192);
            }
            STAMP(12);
            WAITV(0);
            if (arrive(L_CY) == 3) publish(rs, WS_FY, ((b >> 4) & 3) * 64 + (b >> 6) * 16 + (b & 15), 8, e_lo, e_hi, lane);
            STAMP(13);
        }
        {
            const u32 srow = 256 * (4 * (i16 & 1) + v) + 8 * T2 + (i16 >> 1);
            const u32x4* xp = (const u32x4*)(p.x + srow * 512 + 64 * nt + 16 * g);
            const u32x4 xa = xp[0], xb = xp[1];
        u32x4 gf[4][4];
#pragma unroll
        for (int cc = 0; cc < 4; ++cc) {
            const int d = 64 * nt + 16 * (i16 >> 2) + 4 * cc + (i16 & 3);
            const float step = (float)d * (1.f / 512.f);
#pragma unroll
            for (int jj = 0; jj < 4; ++jj) {
                const int kd0 = 16 * (4 * jj + v) + 4 * g;
                const float p0 = (float)((kd0 * d) & 511) * (1.f / 512.f);
#pragma unroll
                for (int j = 0; j < 4; ++j) {
                    const float a = p0 + (float)j * step;
                    gf[jj][cc][j] = pkh(cosr(a), -sinr(a));
                }
            }
            if (ta < 16) gf[0][cc][0] = (i16 & 1) ? 0xb8003800u : 0x38003800u;
#pragma unroll
            for (int jj = 0; jj < 4; ++jj) PIN(gf[jj][cc]);
        }
        SB();
        STAMP(17);
            {
                const u32 fo = WS_FY + (b & 7) * 2048 + ta * 8;
#pragma nounroll
                for (u32 sp = 0; sp < SPIN_CAP; ++sp) {
                    const u32x2 f = ld64(rs, fo, 0);
                    if (all0((f.x ^ e_lo) | (f.y ^ e_hi))) break;
                    __builtin_amdgcn_s_sleep(YSLEEP);
                }
                asm volatile("" ::: "memory");
            }
            STAMP(14);
            u32x4 yl[4][4];
#pragma unroll
            for (int jj = 0; jj < 4; ++jj)
#pragma unroll
                for (int j = 0; j < 4; ++j) yl[jj][j] = ld128(rs, WS_Y + 4 * g * 8192 + i16 * 16, (u32)(16 * (4 * jj + v) + j) * 8192 + T2 * 256);
            SB();
            f32x4 acc[4][4];
#pragma unroll
            for (int c = 0; c < 4; ++c)
#pragma unroll
                for (int cc = 0; cc < 4; ++cc) acc[c][cc] = f32x4{0.f, 0.f, 0.f, 0.f};
#pragma unroll
            for (int jj = 0; jj < 4; ++jj)
#pragma unroll
                for (int c = 0; c < 4; ++c) {
                    const u32x4 yf = {yl[jj][0][c], yl[jj][1][c], yl[jj][2][c], yl[jj][3][c]};
#pragma unroll
                    for (int cc = 0; cc < 4; ++cc) acc[c][cc] = mfma16h(gf[jj][cc], yf, acc[c][cc]);
                }
            STAMP(15);
            wait4(L_FREE, 1u);
#pragma unroll
            for (int k = 1; k < 4; ++k)
#pragma unroll
                for (int cc = 0; cc < 4; ++cc) *(LAS f32x4*)(sm + L_W1A + (v * 3 + k - 1) * 4096 + cc * 1024 + lane * 16) = acc[k][cc];
            post(L_PA + v * 4, 6u); wait4(L_PA, 6u);
            if (b == 0 && ta == 0) { u32x2 e = {e_lo, e_hi}; st64(rs, e, WS_EPOCH, 0); }
            f32x4 o[4];
#pragma unroll
            for (int cc = 0; cc < 4; ++cc) o[cc] = acc[0][cc];
#pragma unroll
            for (int k = 1; k < 4; ++k) {
                const int src = (v - k) & 3;
#pragma unroll
                for (int cc = 0; cc < 4; ++cc) o[cc] += ldsf4(sm, L_W1A + (src * 3 + k - 1) * 4096 + cc * 1024 + lane * 16);
            }
            const float alpha = __builtin_bit_cast(float, wexp << 23) * 0.125f;
            u32x4 z0, z1;
#pragma unroll
            for (int cc = 0; cc < 4; ++cc) {
                const u32 xa_ = cc < 2 ? xa[2 * cc] : xb[2 * cc - 4], xb_ = cc < 2 ? xa[2 * cc + 1] : xb[2 * cc - 3];
                const u32 ya = pkb(o[cc][0] * alpha, o[cc][1] * alpha), yb = pkb(o[cc][2] * alpha, o[cc][3] * alpha);
                const u32 ra = pkb(blo(xa_) + blo(ya), bhi(xa_) + bhi(ya)), rb = pkb(blo(xb_) + blo(yb), bhi(xb_) + bhi(yb));
                if (cc < 2) { z0[2 * cc] = ra; z0[2 * cc + 1] = rb; } else { z1[2 * cc - 4] = ra; z1[2 * cc - 3] = rb; }
            }
            *(LAS u32x4*)(sm + L_SCR + v * 2048 + lane * 16) = z0;
            *(LAS u32x4*)(sm + L_SCR + v * 2048 + 1024 + lane * 16) = z1;
            STAMP(16);
            BAR();
            STAMP_OUT();
        }
    }
}

#if defined(__HIP_DEVICE_COMPILE__)
#pragma clang attribute pop
#endif

extern "C" void kernel_launch(void* const* d_in, const int* in_sizes, int n_in,
                              void* d_out, int out_size, void* d_ws, size_t ws_size,
                              hipStream_t stream) {
    Params p;
    p.x = (const u16*)d_in[0]; p.wr = (const u16*)d_in[1]; p.wi = (const u16*)d_in[2];
    p.w1 = (const u16*)d_in[4]; p.w2 = (const u16*)d_in[5];
    p.out = (u16*)d_out; p.ws = (unsigned char*)d_ws;
    fused_kernel<<<256, 512, 0, stream>>>(p);
}
```

```cpp
#include <hip/hip_runtime.h>
#include <stdint.h>

typedef unsigned short u16;
typedef unsigned int u32;
typedef unsigned long long u64;
typedef u32 u32x4 __attribute__((ext_vector_type(4)));
typedef u32 u32x2 __attribute__((ext_vector_type(2)));
typedef short s16x4 __attribute__((ext_vector_type(4)));
typedef float f32x2 __attribute__((ext_vector_type(2)));
typedef float f32x4 __attribute__((ext_vector_type(4)));
typedef float f32x16 __attribute__((ext_vector_type(16)));
typedef __bf16 bf16x8 __attribute__((ext_vector_type(8)));
typedef __bf16 bf16x2 __attribute__((ext_vector_type(2)));
typedef _Float16 f16x8 __attribute__((ext_vector_type(8)));
typedef _Float16 f16x2 __attribute__((ext_vector_type(2)));
#define LAS __attribute__((address_space(3)))
#define DI __device__ __forceinline__

#define STAMPS 0

constexpr u32 WS_EPOCH = 0;
constexpr u32 WS_F1 = 4096, WS_FY = WS_F1 + 16384;
constexpr u32 WS_FH = WS_FY + 16384;
constexpr u32 WS_ST = WS_FH + 8192;
constexpr u32 WS_X = 65536;
constexpr u32 WS_W = WS_X + (2u << 20);
constexpr u32 WS_Y = WS_W + 259u * 8192u;
constexpr u32 WS_H = WS_Y + (2u << 20);
constexpr u32 WS_END = WS_H + 32u * 43u * 4096u;
constexpr u32 WS_STAMP = 16u << 20;
constexpr u32 OOB = 64u << 20;

constexpr u32 L_XS = 0;
constexpr u32 L_W1 = 65536;
constexpr u32 L_SCR = 139264;
constexpr u32 L_ROW = L_SCR + 12288, L_REC = L_SCR;
constexpr u32 L_SM = 159744;
constexpr u32 L_ST = L_SM;
constexpr u32 L_CS = L_SM + 1024;
constexpr u32 L_CNT = L_SM + 2560;
constexpr u32 L_PB = L_CNT;
constexpr u32 L_PA = L_CNT + 16;
constexpr u32 L_SR = L_CNT + 32;
constexpr u32 L_FREE = L_CNT + 48;
constexpr u32 L_C1 = L_CNT + 64, L_CH = L_CNT + 68, L_CY = L_CNT + 72, L_MAXE = L_CNT + 76;
constexpr u32 L_EP = L_CNT + 80;
constexpr u32 L_GH = L_CNT + 88, L_WEXP = L_CNT + 92;
constexpr u32 L_ZR = L_CNT + 96;
constexpr u32 L_DUMMY = L_SM + 3072;
constexpr u32 L_TOTAL = 163840;
DI constexpr u32 w2slot(int pos) {
    return pos < 34 ? pos * 4096 : pos < 36 ? L_SCR + 12288 + (pos - 34) * 4096 : pos < 42 ? (pos - 14) * 4096 : 12 * 4096;
}

#if defined(__HIP_DEVICE_COMPILE__)
#pragma clang attribute push(__attribute__((target("no-packed-fp32-ops"))), apply_to = function)
#endif

struct Params {
    const u16* x; const u16* wr; const u16* wi; const u16* w1; const u16* w2; u16* out; unsigned char* ws;
};

DI f32x4 mfma16h(u32x4 a, u32x4 b, f32x4 c) { return __builtin_amdgcn_mfma_f32_16x16x32_f16(__builtin_bit_cast(f16x8, a), __builtin_bit_cast(f16x8, b), c, 0, 0, 0); }
DI f32x4 mfma16b(u32x4 a, u32x4 b, f32x4 c) { return __builtin_amdgcn_mfma_f32_16x16x32_bf16(__builtin_bit_cast(bf16x8, a), __builtin_bit_cast(bf16x8, b), c, 0, 0, 0); }
DI f32x16 mfma32b(u32x4 a, u32x4 b, f32x16 c) { return __builtin_amdgcn_mfma_f32_32x32x16_bf16(__builtin_bit_cast(bf16x8, a), __builtin_bit_cast(bf16x8, b), c, 0, 0, 0); }
DI u32 pkh(float a, float b) { f32x2 v = {a, b}; return __builtin_bit_cast(u32, __builtin_convertvector(v, f16x2)); }
DI u32 pkb(float a, float b) { f32x2 v = {a, b}; return __builtin_bit_cast(u32, __builtin_convertvector(v, bf16x2)); }
DI float blo(u32 v) { return __builtin_bit_cast(float, v << 16); }
DI float bhi(u32 v) { return __builtin_bit_cast(float, v & 0xffff0000u); }
DI float rbf(float f) { return blo(pkb(f, f)); }
DI float cosr(float rev) { return __builtin_amdgcn_cosf(rev); }
DI float sinr(float rev) { return __builtin_amdgcn_sinf(rev); }
DI float half_sum(float a) {
    u32 u = __builtin_bit_cast(u32, a);
    auto r = __builtin_amdgcn_permlane32_swap(u, u, false, false);
    return __builtin_bit_cast(float, (u32)r[0]) + __builtin_bit_cast(float, (u32)r[1]);
}
DI bool all0(u32 v) { return __builtin_amdgcn_ballot_w64(v != 0u) == 0ull; }
DI u32 umin(u32 a, u32 b) { return a < b ? a : b; }
DI u32 umax(u32 a, u32 b) { return a > b ? a : b; }
#define WAITV(n) asm volatile("s_waitcnt vmcnt(%0)" ::"i"(n) : "memory")
#define WAITL() asm volatile("s_waitcnt lgkmcnt(0)" ::: "memory")
#define BAR() asm volatile("s_waitcnt lgkmcnt(0)\n\ts_barrier" ::: "memory")
#define PIN(x) asm volatile("" : "+v"(x))
#define SB() __builtin_amdgcn_sched_barrier(0)

DI void glds4(u32 v0, u32 v1, u32 v2, u32 v3, const void* sb, u32 lds) {
    asm volatile("s_mov_b32 m0, %5\n\ts_nop 0\n\t"
                 "global_load_lds_dwordx4 %0, %4\n\t"
                 "global_load_lds_dwordx4 %1, %4 offset:1024\n\t"
                 "global_load_lds_dwordx4 %2, %4 offset:2048\n\t"
                 "global_load_lds_dwordx4 %3, %4 offset:3072"
                 ::"v"(v0), "v"(v1), "v"(v2), "v"(v3), "s"(sb), "s"(lds) : "memory");
}
DI void glds3(u32 v0, u32 v1, u32 v2, const void* sb, u32 lds) {
    asm volatile("s_mov_b32 m0, %4\n\ts_nop 0\n\t"
                 "global_load_lds_dwordx4 %0, %3\n\t"
                 "global_load_lds_dwordx4 %1, %3 offset:1024\n\t"
                 "global_load_lds_dwordx4 %2, %3 offset:2048"
                 ::"v"(v0), "v"(v1), "v"(v2), "s"(sb), "s"(lds) : "memory");
}

template <int OFF> DI void glds1(u32 v, const void* sb, u32 lds) {
    asm volatile("s_mov_b32 m0, %2\n\ts_nop 0\n\tglobal_load_lds_dwordx4 %0, %1 offset:%3" ::"v"(v), "s"(sb), "s"(lds), "i"(OFF) : "memory");
}

typedef __amdgpu_buffer_rsrc_t rsrc_t;
DI u32x4 ld128(rsrc_t rs, u32 voff, u32 soff) { return __builtin_amdgcn_raw_buffer_load_b128(rs, voff, soff, 16); }
DI u32x2 ld64(rsrc_t rs, u32 voff, u32 soff) { return __builtin_amdgcn_raw_buffer_load_b64(rs, voff, soff, 16); }
DI void st128(rsrc_t rs, u32x4 v, u32 voff, u32 soff) { u32 o = voff + soff; asm volatile("" : "+v"(o)); __builtin_amdgcn_raw_buffer_store_b128(v, rs, o, 0, 16); }
DI void st64(rsrc_t rs, u32x2 v, u32 voff, u32 soff) { __builtin_amdgcn_raw_buffer_store_b64(v, rs, voff, soff, 16); }

DI u32x2 poll64(u32x4 srd, u32 voff) {
    u32x2 d = {0u, 0u};
    asm volatile("buffer_atomic_add_x2 %0, %1, %2, 0 offen sc0 sc1\n\ts_waitcnt vmcnt(0)" : "+v"(d) : "v"(voff), "s"(srd) : "memory");
    return d;
}
DI void poll64x2(u32x4 srd, u32 v0, u32 v1, u32x2& a, u32x2& b) {
    a = u32x2{0u, 0u}; b = u32x2{0u, 0u};
    asm volatile("buffer_atomic_add_x2 %0, %2, %4, 0 offen sc0 sc1\n\tbuffer_atomic_add_x2 %1, %3, %4, 0 offen sc0 sc1\n\ts_waitcnt vmcnt(0)" : "+v"(a), "+v"(b) : "v"(v0), "v"(v1), "s"(srd) : "memory");
}

typedef LAS unsigned char* lds_t;
DI u32x4 lds128(lds_t sm, u32 off) { return *(LAS u32x4*)(sm + off); }
DI f32x4 ldsf4(lds_t sm, u32 off) { return *(LAS f32x4*)(sm + off); }
DI u32x2 ldstr(lds_t sm, u32 off) { return __builtin_bit_cast(u32x2, __builtin_amdgcn_ds_read_tr16_b64_v4i16((LAS s16x4*)(sm + off))); }
DI u32x4 cat(u32x2 a, u32x2 b) { u32x4 r = {a.x, a.y, b.x, b.y}; return r; }

constexpr u32 SPIN_CAP = 1u << 16;

DI void post(u32 addr, u32 val) {
    asm volatile("s_mov_b64 exec, 1\n\tds_write_b32 %0, %1\n\ts_mov_b64 exec, -1" ::"v"(addr), "v"(val) : "memory");
}
DI void wait4(u32 addr, u32 target) {
#pragma nounroll
    for (u32 sp = 0; sp < (SPIN_CAP << 4); ++sp) {
        u32x4 v;
        asm volatile("ds_read_b128 %0, %1\n\ts_waitcnt lgkmcnt(0)" : "=&v"(v) : "v"(addr) : "memory");
        const u32 m = umin(umin(v.x, v.y), umin(v.z, v.w));
        if ((u32)__builtin_amdgcn_readfirstlane(m) >= target) break;
    }
}
DI u32 wait1(u32 addr, bool nap) {
    u32 r = 0;
#pragma nounroll
    for (u32 sp = 0; sp < (SPIN_CAP << 4); ++sp) {
        u32 v;
        asm volatile("ds_read_b32 %0, %1\n\ts_waitcnt lgkmcnt(0)" : "=&v"(v) : "v"(addr) : "memory");
        r = __builtin_amdgcn_readfirstlane(v);
        if (r != 0u) break;
        if (nap) __builtin_amdgcn_s_sleep(1);
    }
    return r;
}
DI u32 arrive(u32 addr) {
    u32 old;
    asm volatile("s_mov_b64 exec, 1\n\tds_add_rtn_u32 %0, %1, %2\n\ts_waitcnt lgkmcnt(0)\n\ts_mov_b64 exec, -1" : "=&v"(old) : "v"(addr), "v"(1u) : "memory");
    return __builtin_amdgcn_readfirstlane(old);
}
DI void publish(rsrc_t rs, u32 base, int slot, int R, u32 lo, u32 hi, int lane) {
    u32x2 v = {lo, hi}; st64(rs, v, lane < R ? base + lane * 2048 : OOB, slot * 8);
}

#define STAMP(i) do {} while (0)
#define STAMPR(i) do {} while (0)
#define STAMP_OUT() do {} while (0)
#define PF_W 1
#define XDELAY 16
#define XSLEEP 16
#define HDELAY 0
#define APOLL 0
#define YDELAY 16
#define APRIO 0
#define PSLEEP 4
#define YSLEEP 16
#define PF_LINES 280

DI float wave_sum(float x) {
    int v = __builtin_bit_cast(int, x);
#define DPPADD(ctrl, rm) x += __builtin_bit_cast(float, __builtin_amdgcn_update_dpp(0, __builtin_bit_cast(int, x), ctrl, rm, 0xf, true))
    DPPADD(0xB1, 0xf);
    DPPADD(0x4E, 0xf);
    DPPADD(0x141, 0xf);
    DPPADD(0x140, 0xf);
    DPPADD(0x142, 0xa);
    DPPADD(0x143, 0xc);
    (void)v;
    return __builtin_bit_cast(float, __builtin_amdgcn_readlane(__builtin_bit_cast(int, x), 63));
}

__global__ __launch_bounds__(512, 2) void fused_kernel(Params p) {
    __shared__ __attribute__((aligned(16))) unsigned char smem[L_TOTAL];
    lds_t sm = (lds_t)smem;
    const int tid = threadIdx.x, lane = tid & 63;
    const int wave = __builtin_amdgcn_readfirstlane(tid >> 6);
    const int b = blockIdx.x;
    const int xr = b & 3, xc = (b >> 2) & 1, idx = b >> 3;
    const int T = 4 * xr + (idx >> 3), ct = 8 * xc + (idx & 7);
    const int T2 = 4 * (b & 7) + (idx >> 3), nt = idx & 7;
    const int rot = ((idx & 7) + 2 * (idx >> 3)) & 7;
    const int blk0 = ct < 14 ? 3 * ct : 40;
    const int l31 = lane & 31, hh = lane >> 5, i16 = lane & 15, g = lane >> 4;
    if (PF_LINES > 0) {
        const u64 pc = __builtin_amdgcn_s_getpc() & ~(u64)255;
        if (wave == 3) {
            constexpr int PER = (PF_LINES + 31) / 32;
            u32 ln = idx * PER + (lane < PER ? lane : 0); ln = ln < PF_LINES ? ln : 0;
            asm volatile("s_mov_b32 m0, %2\n\ts_nop 0\n\tglobal_load_lds_dword %0, %1" ::"v"(ln * 128), "s"(pc), "s"(L_DUMMY) : "memory");
        }
    }
    if (wave == 2) {
        const u32 ln = b * 43 + (lane < 43 ? lane : 0);
        asm volatile("s_mov_b32 m0, %2\n\ts_nop 0\n\tglobal_load_lds_dword %0, %1" ::"v"(ln * 128), "s"(p.w2), "s"(L_DUMMY) : "memory");
    }
    STAMPR(29);
    STAMP(0);

    if (wave < 4) {
        const int w = wave;
        if (tid < 32) *(LAS u32*)(sm + L_CNT + tid * 4) = 0u;
        const u32 xrow = (256 * (lane >> 3) + 16 * T + 4 * w) * 1024;
        const u32 xo_e = xrow + (((lane & 7) ^ g) << 4), xo_o = xrow + (((lane & 7) ^ (4 + g)) << 4);
        const u32 xs = L_XS + w * 16384;
        u32 wv0, wv1, wv2;
        { u32 q = lane, r = q / 12u; wv0 = r * 2752 + (q - r * 12u) * 16; q += 64; r = q / 12u; wv1 = r * 2752 + (q - r * 12u) * 16 - 1024; q += 64; r = q / 12u; wv2 = r * 2752 + (q - r * 12u) * 16 - 2048; }
        auto piece = [&](int j, int i) {
            if (i < 4) {
                const char* sb = (const char*)p.x + ((j + rot) & 7) * 128; const u32 slot = xs + (j & 3) * 4096;
                if (i == 0) glds1<0>(xo_e, sb, slot); else if (i == 1) glds1<1024>(xo_o, sb, slot); else if (i == 2) glds1<2048>(xo_e, sb, slot); else glds1<3072>(xo_o, sb, slot);
            } else {
                const u32 slot = L_W1 + (j % 6) * 12288 + w * 3072;
                const char* sb = (const char*)p.w1 + (64 * ((j + rot) & 7) + 16 * w) * 2752 + blk0 * 64;
                if (i == 4) glds1<0>(wv0, sb, slot); else if (i == 5) glds1<1024>(wv1, sb, slot); else glds1<2048>(wv2, sb, slot);
            }
        };
        u32 xrd[4], wa[4];
        {
            const u32 w1l = (8 * hh + (i16 >> 2)) * 192 + (g & 1) * 32 + (lane & 3) * 8;
#pragma unroll
            for (int t = 0; t < 4; ++t) {
                const int tr = (t + w) & 3;
                xrd[t] = xs + l31 * 128 + (((2 * tr + hh) ^ ((l31 >> 1) & 7)) << 4);
                wa[t] = w1l + L_W1 + tr * 3072; PIN(wa[t]); PIN(xrd[t]);
            }
        }
        const u32 one = ((g ^ i16) & 1) ? 0u : 0x3f803f80u;
        u32x4 pones = {one, one, one, one}; PIN(pones);
        u32 w2v = (8 * w + (lane >> 3)) * 1024 + (((lane & 7) ^ (((g & 2) << 1) | (g & 1))) << 4); PIN(w2v);
        auto w2piece = [&](int pos) { glds1<0>(w2v, (const char*)p.w2 + pos * 32768 + nt * 128, w2slot(pos) + w * 1024); };
        BAR();
#pragma unroll
        for (int j = 0; j < 3; ++j)
#pragma unroll
            for (int i = 0; i < 7; ++i) piece(j, i);
        STAMP(1);
        const rsrc_t rs = __builtin_amdgcn_make_buffer_rsrc(p.ws, 0, WS_END, 0x00020000);
        const u32x4 srd = {(u32)(uintptr_t)p.ws, (u32)((uintptr_t)p.ws >> 32) & 0xffffu, WS_END, 0x00020000u};
        f32x16 acc1[3];
        f32x4 accc[3];
#pragma unroll
        for (int n = 0; n < 3; ++n) {
            accc[n] = f32x4{0.f, 0.f, 0.f, 0.f};
#pragma unroll
            for (int r = 0; r < 16; ++r) acc1[n][r] = 0.f;
        }
        {
            u32x4 xf[3], wf[3][3];
            auto xread = [&](int s) { xf[s % 3] = lds128(sm, xrd[s & 3] + ((s >> 2) & 3) * 4096); };
            auto w1read = [&](int s, int n) { const u32 a = wa[s & 3] + ((s >> 2) % 6) * 12288 + n * 64; wf[s % 3][n] = cat(ldstr(sm, a), ldstr(sm, a + 768)); };
            auto issue = [&](int j, int i) {
                if (j < 5) piece(j + 3, i);
                else if (j == 5) { if (i < 3) w2piece(36 + i); }
                else if (j == 6) w2piece(i < 4 ? 39 + i : 4 * (i - 4));
                else w2piece(i < 3 ? 28 + i : 4 * (i - 3) + 1);
            };
            WAITV(14); post(L_PB + w * 4, 1u);
            xread(0);
#pragma unroll
            for (int n = 0; n < 3; ++n) w1read(0, n);
            wait4(L_PB, 1u);
            STAMP(2);
            xread(1);
#pragma unroll
            for (int n = 0; n < 3; ++n) w1read(1, n);
            u32x4 pv = {0u, 0u, 0u, 0u};
#pragma unroll
            for (int s = 0; s < 32; ++s) {
                const int j = s >> 2, t = s & 3;
                if (j < 7 && t == 2) {
                    if (j < 5) WAITV(11); else if (j == 5) WAITV(10); else WAITV(7);
                    post(L_PB + w * 4, j + 2);
                }
                if (j < 7 && t == 3) {
                    const u32 m = umin(umin(pv.x, pv.y), umin(pv.z, pv.w));
                    if (__builtin_expect((u32)__builtin_amdgcn_readfirstlane(m) < (u32)(j + 2), 0)) wait4(L_PB, j + 2);
                    asm volatile("" ::: "memory");
                }
#pragma unroll
                for (int n = 0; n < 3; ++n) {
                    SB();
                    acc1[n] = mfma32b(wf[s % 3][n], xf[s % 3], acc1[n]);
                    SB();
                    if (n == 1 && j < 7 && t == 2) pv = *(volatile LAS u32x4*)(sm + L_PB);
                    if (s + 2 < 32) { if (n == 0) xread(s + 2); w1read(s + 2, n); }
                    if (n == 0 || (n == 2 && t < 3)) issue(j, 2 * t + (n >> 1));
                }
                SB();
                if (t == 0) {
#pragma unroll
                    for (int n = 0; n < 3; ++n) accc[n] = mfma16b(pones, wf[s % 3][n], accc[n]);
                }
                SB();
                if (s == 15) STAMP(3);
                if (s == 27) STAMP(4);
            }
        }
        STAMP(5);
        float rstd;
        {
#pragma unroll
            for (int n = 0; n < 3; ++n) {
                const float c = (lane & 16) ? accc[n][1] : accc[n][0];
                const u32 h2 = pkb(c, c);
                const u32 pk = pkb(blo(h2), c - blo(h2));
                *(LAS u32*)(sm + L_CS + (n * 32 + l31) * 16 + w * 4) = pk;
            }
            post(L_PB + w * 4, 9u);
            wait4(L_SR, 1u);
            const u32x2 st = *(LAS u32x2*)(sm + L_ST + w * 256 + l31 * 8);
            wait4(L_PB, 9u);
            const u32 negm = hh ? 0u : st.x;
            { const u32 sy = st.y; rstd = __builtin_bit_cast(float, sy); }
            const u32x4 nm = {negm, negm, negm, negm};
#pragma unroll
            for (int n = 0; n < 3; ++n) acc1[n] = mfma32b(lds128(sm, L_CS + (n * 32 + l31) * 16), nm, acc1[n]);
        }
        const u32 e_lo = __builtin_amdgcn_readfirstlane(*(LAS u32*)(sm + L_EP)), e_hi = __builtin_amdgcn_readfirstlane(*(LAS u32*)(sm + L_EP + 4));
        STAMP(6);
        {
            const u32 ho = WS_H + (u32)((2 * T + (w >> 1)) * 43) * 4096 + (l31 & 3) * 1024 + hh * 256 + (8 * (w & 1) + (l31 >> 2)) * 16;
            const float rs2 = rstd * -1.44269504089f;
#pragma unroll
            for (int n = 0; n < 3; ++n) {
                u32 pk[8];
#pragma unroll
                for (int q = 0; q < 8; ++q) {
                    if ((q & 1) == 0) { const int e = 4 * n + (q >> 1); SB(); w2piece(e < 8 ? 4 * (e >> 1) + 2 + (e & 1) : 8 + e); SB(); }
                    const float a0 = acc1[n][2 * q], a1 = acc1[n][2 * q + 1];
                    const float s0 = a0 * rstd * __builtin_amdgcn_rcpf(1.f + __builtin_amdgcn_exp2f(a0 * rs2));
                    const float s1 = a1 * rstd * __builtin_amdgcn_rcpf(1.f + __builtin_amdgcn_exp2f(a1 * rs2));
                    pk[q] = pkb(s0, s1);
                }
                const u32 hv = (ct < 14 || (ct == 14 && n == 2)) ? ho : OOB;
                u32x4 a = {pk[0], pk[1], pk[2], pk[3]}, c = {pk[4], pk[5], pk[6], pk[7]};
                st128(rs, a, hv, (u32)(blk0 + n) * 4096);
                st128(rs, c, hv + 512, (u32)(blk0 + n) * 4096);
                SB();
            }
        }
        STAMP(7);
        w2piece(20); w2piece(21);
#pragma unroll
        for (int pos = 31; pos < 36; ++pos) w2piece(pos);
        STAMP(8);
        WAITV(7);
        if (arrive(L_CH) == 3) publish(rs, WS_FH, T * 16 + ct, 4, e_lo, e_hi, lane);
        post(L_PB + w * 4, 10u);
        STAMP(9);
        const int mh = w & 1, kh = w >> 1;
        u32 rd[3][4];
        {
            const u32 q = i16 >> 2, pp = lane & 3;
            const u32 rrow = (16 * (g >> 1) + 4 * (g & 1) + q) * 128 + kh * 4096;
            const u32 sw = ((g & 1) << 2) | (q >> 1);
#pragma unroll
            for (int cc = 0; cc < 4; ++cc) {
                rd[0][cc] = rrow + (((4 * (cc >> 1) + pp) ^ sw) << 4) + (((cc & 1) ^ (pp & 1)) << 3);
                rd[1][cc] = rd[0][cc] + 57344; rd[2][cc] = rd[0][cc] + 114688;
                PIN(rd[0][cc]); PIN(rd[1][cc]); PIN(rd[2][cc]);
            }
        }
        auto w2frag = [&](int q, int cc) -> u32x4 {
            const u32 sl = w2slot(2 * q), win = sl / 57344, a = rd[win][cc] + (sl - win * 57344);
            return cat(ldstr(sm, a), ldstr(sm, a + 1024));
        };
        u32x4 wreg[4][4];
        wait4(L_PB, 10u);
#pragma unroll
        for (int q = 0; q < 4; ++q)
#pragma unroll
            for (int cc = 0; cc < 4; ++cc) wreg[q][cc] = w2frag(18 + q, cc);
        post(L_PB + w * 4, 11u); wait4(L_PB, 11u);
        w2piece(12);
#pragma unroll
        for (int pos = 22; pos < 28; ++pos) w2piece(pos);
        STAMP(10);
        if (w == 0) {
            const u32 fo = lane < 15 ? WS_FH + (idx & 3) * 2048 + (T2 >> 1) * 128 + lane * 8 : OOB;
#pragma nounroll
            for (u32 sp = 0; sp < SPIN_CAP; ++sp) {
                const u32x2 f = poll64(srd, fo);
                if (all0(lane < 15 ? (f.x ^ e_lo) | (f.y ^ e_hi) : 0u)) break;
                __builtin_amdgcn_s_sleep(PSLEEP);
            }
        } else {
            WAITV(0);
        }
        post(L_PB + w * 4, 12u); wait4(L_PB, 12u);
        STAMP(11);
        f32x4 acc[2][4];
#pragma unroll
        for (int c = 0; c < 2; ++c)
#pragma unroll
            for (int cc = 0; cc < 4; ++cc) acc[c][cc] = f32x4{0.f, 0.f, 0.f, 0.f};
        const u32 hb = WS_H + (u32)(T2 * 43 + kh) * 4096;
        const u32 hv0 = lane * 16 + (2 * mh + kh) * 1024, hv1 = lane * 16 + (2 * mh + (kh ^ 1)) * 1024;
        u32x4 hf[22][2];
        auto hload = [&](int q, int c) {
            const u32 v_ = c ? hv1 : hv0;
            hf[q][c] = ld128(rs, (q == 21 && kh) ? OOB : v_, hb + q * 8192);
        };
        constexpr int HD = 8;
#pragma unroll
        for (int q = 0; q < HD; ++q) { hload(q, 0); hload(q, 1); }
        u32x4 wf[2][4];
#pragma unroll
        for (int cc = 0; cc < 4; ++cc) wf[0][cc] = w2frag(0, cc);
        SB();
#pragma unroll
        for (int q = 0; q < 22; ++q) {
#pragma unroll
            for (int c = 0; c < 2; ++c) {
#pragma unroll
                for (int cc = 0; cc < 4; ++cc) {
                    acc[c][cc] = mfma16b(q < 18 ? wf[q & 1][cc] : wreg[q - 18][cc], hf[q][c], acc[c][cc]);
                    SB();
                    if (q + 1 < 18 && (cc & 1) == 0) wf[(q + 1) & 1][2 * c + (cc >> 1)] = w2frag(q + 1, 2 * c + (cc >> 1));
                    SB();
                }
                if (q + HD < 22) hload(q + HD, c);
                SB();
            }
            if (q == 4) post(L_FREE + w * 4, 1u);
            if (q == 10) post(L_PB + w * 4, 13u);
            if (q == 18) wait4(L_PB, 13u);
            if (q == 0) STAMP(12);
            SB();
        }
        STAMP(13);
        {
            const u32 ex = 16 * 4096 + lane * 16;
#pragma unroll
            for (int cc = 0; cc < 4; ++cc) *(LAS f32x4*)(sm + ex + w * 4096 + cc * 1024) = acc[1][cc];
            post(L_PB + w * 4, 14u);
            const int cls = 2 * mh + kh;
            wait1(L_ZR + cls * 4, false);
            const u32x4 z0 = lds128(sm, L_SCR + cls * 2048 + lane * 16), z1 = lds128(sm, L_SCR + cls * 2048 + 1024 + lane * 16);
            wait4(L_PB, 14u);
            STAMP(14);
            u32 r[8];
#pragma unroll
            for (int cc = 0; cc < 4; ++cc) {
                const f32x4 o = acc[0][cc] + ldsf4(sm, ex + (w ^ 2) * 4096 + cc * 1024);
#pragma unroll
                for (int e = 0; e < 2; ++e) {
                    const u32 z = cc < 2 ? z0[2 * cc + e] : z1[2 * cc - 4 + e];
                    const u32 f = pkb(o[2 * e], o[2 * e + 1]);
                    r[2 * cc + e] = pkb(blo(z) + blo(f), bhi(z) + bhi(f));
                }
            }
            const bool odd = g & 1;
            const u32x4 o0 = {odd ? r[2] : r[0], odd ? r[3] : r[1], odd ? r[0] : r[2], odd ? r[1] : r[3]};
            const u32x4 o1 = {odd ? r[6] : r[4], odd ? r[7] : r[5], odd ? r[4] : r[6], odd ? r[5] : r[7]};
            const u32 srow = 256 * (4 * (i16 & 1) + cls) + 8 * T2 + (i16 >> 1);
            u32x4* op = (u32x4*)(p.out + srow * 512 + 64 * nt + 8 * g);
            op[0] = o0; op[4] = o1;
        }
        STAMP(15);
        STAMP_OUT();
    } else {
        const int v = wave - 4, ta = tid - 256;
        const u32x4 xa = *(const u32x4*)(p.x + (512 * v + b) * 512 + lane * 8), xb = *(const u32x4*)(p.x + (512 * v + 256 + b) * 512 + lane * 8);
        const rsrc_t rs = __builtin_amdgcn_make_buffer_rsrc(p.ws, 0, WS_END, 0x00020000);
        const u32x2 eld = ld64(rs, WS_EPOCH, 0);
        SB();
        const rsrc_t rwr = __builtin_amdgcn_make_buffer_rsrc((void*)p.wr, 0, 2048 * 257 * 2, 0x00020000);
        const rsrc_t rwi = __builtin_amdgcn_make_buffer_rsrc((void*)p.wi, 0, 2048 * 257 * 2, 0x00020000);
        u32 wre[4], wim[4], wre2, wim2;
        const int ks0 = 2 * (b >> 6) + 8 * (b & 14) + 512 * ((b >> 4) & 3);
        const int wcol = 128 * (b & 1) + 32 * v + 2 * (lane >> 2);
        {
            const u32 vo = (8 * ((lane >> 1) & 1) + (lane & 1)) * 514 + wcol * 2;
#pragma unroll
            for (int r = 0; r < 4; ++r) {
                wre[r] = __builtin_amdgcn_raw_buffer_load_b32(rwr, vo, (ks0 + 128 * r) * 514, 0);
                wim[r] = __builtin_amdgcn_raw_buffer_load_b32(rwi, vo, (ks0 + 128 * r) * 514, 0);
            }
            const int e = lane & 7;
            const u32 eo = ta < 24 ? ((e >> 2) + 128 * (e & 3) + 8 * (b & 1)) * 514 + ((lane >> 3) == 1 ? 0 : 512) : 0x40000000u;
            wre2 = __builtin_amdgcn_raw_buffer_load_b16(rwr, eo, ks0 * 514, 0);
            wim2 = __builtin_amdgcn_raw_buffer_load_b16(rwi, eo, ks0 * 514, 0);
        }
        SB();
        __builtin_amdgcn_s_setprio(APRIO);
        BAR();
        STAMP(1);
        const f32x4 zero = {0.f, 0.f, 0.f, 0.f};
        u32x4 f16r, f16i, rc[2], rsn[2], rsp[2];
        float twc[2][4], tws[2][4];
        u32 reca[2][4];
        {
            float c[4], s[4];
#pragma unroll
            for (int j = 0; j < 4; ++j) { const float a = (float)(((4 * g + j) * i16) & 15) * (1.f / 16.f); c[j] = 0.25f * cosr(a); s[j] = 0.25f * sinr(a); }
            f16r = u32x4{pkh(c[0], c[1]), pkh(c[2], c[3]), pkh(s[0], s[1]), pkh(s[2], s[3])};
            f16i = u32x4{f16r.z ^ 0x80008000u, f16r.w ^ 0x80008000u, f16r.x, f16r.y};
            const float st = (float)i16 * (1.f / 32.f), p0 = (float)((8 * g * i16) & 31) * (1.f / 32.f);
#pragma unroll
            for (int j = 0; j < 4; ++j) {
                const float a0 = p0 + st * (float)(2 * j), a1 = p0 + st * (float)(2 * j + 1);
                rc[0][j] = pkh(0.125f * cosr(a0), 0.125f * cosr(a1)); rsp[0][j] = pkh(0.125f * sinr(a0), 0.125f * sinr(a1));
                rsn[0][j] = rsp[0][j] ^ 0x80008000u;
                rc[1][j] = rc[0][j] ^ 0x80000000u; rsp[1][j] = rsp[0][j] ^ 0x80000000u; rsn[1][j] = rsp[0][j] ^ 0x00008000u;
            }
#pragma unroll
            for (int hf = 0; hf < 2; ++hf) {
#pragma unroll
                for (int r = 0; r < 4; ++r) {
                    const float a = (float)((4 * g + r) * (i16 + 16 * hf)) * (1.f / 512.f);
                    twc[hf][r] = cosr(a); tws[hf][r] = sinr(a); PIN(twc[hf][r]); PIN(tws[hf][r]);
                    const int kd = i16 + 16 * hf + 32 * (4 * g + r);
                    reca[hf][r] = L_REC + (2 * v + (g >> 1)) * 1088 + ((g < 2 ? kd : 512 - kd) & 255) * 4; PIN(reca[hf][r]);
                }
                PIN(rc[hf]); PIN(rsn[hf]); PIN(rsp[hf]);
            }
            PIN(f16r); PIN(f16i);
        }
        SB();
        STAMP(2);
        u32 e_lo, e_hi, gran[2];
        {
            const u64 e = (((u64)eld.y << 32) | eld.x) + 1ull;
            e_lo = __builtin_amdgcn_readfirstlane((u32)e); e_hi = __builtin_amdgcn_readfirstlane((u32)(e >> 32));
#pragma unroll
            for (int k = 0; k < 2; ++k) {
                const u32x4 xv = k ? xb : xa;
                float f[8];
#pragma unroll
                for (int j = 0; j < 4; ++j) { f[2 * j] = blo(xv[j]); f[2 * j + 1] = bhi(xv[j]); }
                float s1 = ((f[0] + f[1]) + (f[2] + f[3])) + ((f[4] + f[5]) + (f[6] + f[7]));
                float s2 = f[0] * f[0];
#pragma unroll
                for (int j = 1; j < 8; ++j) s2 = __builtin_fmaf(f[j], f[j], s2);
                const float mean = wave_sum(s1) * (1.f / 512.f);
                float var = wave_sum(s2) * (1.f / 512.f) - mean * mean;
                var = var < 0.f ? 0.f : var;
                const float mb = rbf(mean);
                const float sd = rbf(__builtin_amdgcn_sqrtf(rbf(rbf(var) + 1e-5f)));
                const float ri = __builtin_amdgcn_rcpf(sd);
                gran[k] = (__builtin_bit_cast(u32, mb) >> 16) | __builtin_bit_cast(u32, sd);
                u32x4 o;
#pragma unroll
                for (int j = 0; j < 4; ++j) {
                    const u32 d = pkb(f[2 * j] - mb, f[2 * j + 1] - mb);
                    const u32 n = pkb(blo(d) * ri, bhi(d) * ri);
                    o[j] = pkh(blo(n), bhi(n));
                }
                *(LAS u32x4*)(sm + L_ROW + (2 * v + k) * 1024 + lane * 16) = o;
            }
        }
        STAMP(3);
        {
            const u32 ra = L_ROW + 2 * v * 1024 + g * 256 + i16 * 8;
            const u32x4 zr = cat(ldstr(sm, ra), ldstr(sm, ra + 128)), zi = cat(ldstr(sm, ra + 1024), ldstr(sm, ra + 1152));
#pragma unroll
            for (int hf = 0; hf < 2; ++hf) {
                const f32x4 ur = mfma16h(zi, rsp[hf], mfma16h(zr, rc[hf], zero)), ui = mfma16h(zr, rsn[hf], mfma16h(zi, rc[hf], zero));
                float a[4], c[4];
#pragma unroll
                for (int r = 0; r < 4; ++r) { a[r] = ur[r] * twc[hf][r] + ui[r] * tws[hf][r]; c[r] = ui[r] * twc[hf][r] - ur[r] * tws[hf][r]; }
                const u32x4 d2 = {pkh(a[0], a[1]), pkh(a[2], a[3]), pkh(c[0], c[1]), pkh(c[2], c[3])};
                const f32x4 or_ = mfma16h(f16r, d2, zero), oi = mfma16h(f16i, d2, zero);
#pragma unroll
                for (int r = 0; r < 4; ++r) *(LAS u32*)(sm + reca[hf][r]) = pkh(or_[r], oi[r]);
            }
            post(L_PA + v * 4, 1u);
        }
        wait4(L_PA, 1u);
        STAMP(4);
        {
#pragma unroll
            for (int k = 0; k < 2; ++k) {
                const u32 pr = 32 * v + 16 * k + (lane >> 2);
                const u32 kd = (pr & 7) | ((lane & 2) << 2) | ((pr >> 3) << 4), a = L_REC + (lane & 1) * 4352 + kd * 4;
                const u32x4 o = {*(LAS u32*)(sm + a), *(LAS u32*)(sm + a + 1088), *(LAS u32*)(sm + a + 2176), *(LAS u32*)(sm + a + 3264)};
                st128(rs, o, WS_X + pr * 16384 + (lane & 3) * 16, b * 64);
            }
            st64(rs, u32x2{(lane & 1) ? gran[1] : gran[0], e_lo}, lane < 2 ? WS_ST + (2 * v + lane) * 8 : OOB, b * 64);
            SB();
            STAMP(5);
            {
                u32x4 a, c;
                const u32 w256 = wre2 | (wim2 << 16);
                typedef unsigned short us2 __attribute__((ext_vector_type(2)));
                us2 mx = __builtin_bit_cast(us2, w256 & 0x7f807f80u);
#pragma unroll
                for (int r = 0; r < 4; ++r) {
                    a[r] = __builtin_amdgcn_perm(wim[r], wre[r], 0x05040100u);
                    c[r] = __builtin_amdgcn_perm(wim[r], wre[r], 0x07060302u);
                    mx = __builtin_elementwise_max(mx, __builtin_bit_cast(us2, wre[r] & 0x7f807f80u));
                    mx = __builtin_elementwise_max(mx, __builtin_bit_cast(us2, wim[r] & 0x7f807f80u));
                }
                st128(rs, a, WS_W + wcol * 8192 + (lane & 3) * 16, (b & ~1) * 32);
                st128(rs, c, WS_W + wcol * 8192 + 8192 + (lane & 3) * 16, (b & ~1) * 32);
                const int e2 = lane & 7, ks = ks0 + 8 * (b & 1) + (e2 >> 2) + 128 * (e2 & 3);
                const int km = lane < 8 ? ks : ((2048 - ks) & 2047);
                const u32 mo = (u32)(((km & 7) >> 1) * 64 + (km >> 9) * 16 + ((km >> 3) & 15)) * 32 + (km & 1) * 16 + ((km >> 7) & 3) * 4;
                __builtin_amdgcn_raw_buffer_store_b32(w256, rs, ta < 24 ? WS_W + (256 + (lane >> 3)) * 8192 + mo : OOB, 0, 16);
                const u32 m1 = (u32)(mx.x > mx.y ? mx.x : mx.y) >> 7;
                asm volatile("ds_max_u32 %0, %1" ::"v"(L_MAXE), "v"(m1) : "memory");
            }
        }
        SB();
        STAMP(6);
        u32x4 f8a0, f8a1, f8d0, f8d1, c1r[2], c1i[2], c4r[2], c4i[2];
        float t1c[2][4], t1s[2][4], t3c[2][4], t3s[2][4];
        {
            {
                const float ph = 0.25f * (float)(i16 & 1), st = (float)(i16 >> 1) * 0.125f;
                u32x4 f;
#pragma unroll
                for (int q = 0; q < 2; ++q) {
                    const float t0 = ph + st * (float)(4 * (g & 1) + 2 * q), t1 = t0 + st;
                    const float a0 = cosr(t0), b0 = sinr(t0), a1 = cosr(t1), b1 = sinr(t1);
                    const float k0 = b == 0 ? 0.25f : 0.125f, k1 = b == 0 ? 0.f : 0.125f, k2 = b == 0 ? 0.25f : 0.f;
                    f[2 * q] = pkh(k0 * a0 - k1 * b1, k0 * a1 + k1 * b0);
                    f[2 * q + 1] = pkh(k1 * (a0 + b1) + k2 * b0, k1 * (a1 - b0) + k2 * b1);
                }
                const u32x4 z = {0u, 0u, 0u, 0u};
                f8a0 = g < 2 ? f : z; f8a1 = g < 2 ? z : f;
            }
            {
                const float ph = 0.25f * (float)((g & 1) - (i16 & 1));
                const int vr = (b >> 4) & 3, c0 = ((i16 >> 1) & 1) + vr;
                const float amp = ((g >> 1) == (i16 >> 3)) ? 0.5f : 0.f;
                const float st0 = (float)(4 * ((i16 >> 2) & 1) + (c0 & 3)) * 0.125f, st1 = (float)(4 * ((i16 >> 2) & 1) + ((c0 + 2) & 3)) * 0.125f;
#pragma unroll
                for (int j = 0; j < 4; ++j) {
                    f8d0[j] = pkh(amp * cosr(ph + st0 * (float)(2 * j)), amp * cosr(ph + st0 * (float)(2 * j + 1)));
                    f8d1[j] = pkh(amp * cosr(ph + st1 * (float)(2 * j)), amp * cosr(ph + st1 * (float)(2 * j + 1)));
                }
            }
            PIN(f8a0); PIN(f8a1); PIN(f8d0); PIN(f8d1);
            SB();
            STAMP(7);
            WAITV(0);
            if (arrive(L_C1) == 3) {
                const u32 mx = *(LAS u32*)(sm + L_MAXE);
                publish(rs, WS_F1, b, 8, (e_lo << 8) | mx, (e_hi << 8) | (e_lo >> 24), lane);
            }
            STAMP(8);
            const u32 so = WS_ST + ((16 * T + 4 * v) * 8 + l31) * 8;
            u32x2 sg;
#pragma unroll
            for (int u = 0; u < 2; ++u) {
                const int ka = 2 * v + u;
                float c[4], s[4];
#pragma unroll
                for (int j = 0; j < 4; ++j) {
                    const float a2 = (float)((4 * g + j) * (8 * i16 + ka)) * (1.f / 2048.f);
                    c[j] = 0.25f * cosr(a2 * 16.f); s[j] = 0.25f * sinr(a2 * 16.f);
                    t1c[u][j] = cosr(a2); t1s[u][j] = sinr(a2);
                }
                c1r[u] = u32x4{pkh(c[0], c[1]), pkh(c[2], c[3]), pkh(s[0], s[1]), pkh(s[2], s[3])};
                c1i[u] = u32x4{c1r[u].z ^ 0x80008000u, c1r[u].w ^ 0x80008000u, c1r[u].x, c1r[u].y};
#pragma unroll
                for (int j = 0; j < 4; ++j) {
                    const float a2 = (float)(i16 * (8 * (4 * g + j) + ka)) * (1.f / 2048.f);
                    c[j] = 0.25f * cosr(a2 * 16.f); s[j] = 0.25f * sinr(a2 * 16.f);
                    t3c[u][j] = cosr(a2); t3s[u][j] = sinr(a2);
                }
                c4i[u] = u32x4{pkh(s[0], s[1]), pkh(s[2], s[3]), pkh(c[0], c[1]), pkh(c[2], c[3])};
                c4r[u] = u32x4{c4i[u].z, c4i[u].w, c4i[u].x ^ 0x80008000u, c4i[u].y ^ 0x80008000u};
                PIN(c1r[u]); PIN(c1i[u]); PIN(c4r[u]); PIN(c4i[u]);
#pragma unroll
                for (int j = 0; j < 4; ++j) { PIN(t1c[u][j]); PIN(t1s[u][j]); PIN(t3c[u][j]); PIN(t3s[u][j]); }
                SB();
                if (u == 0) { sg = ld64(rs, so, 0); SB(); STAMP(9); }
                if (u == 1) {
#pragma nounroll
                    for (u32 sp = 0; sp < SPIN_CAP; ++sp) {
                        if (__builtin_expect(all0(sg.y ^ e_lo), 1)) break;
                        __builtin_amdgcn_s_sleep(8);
                        sg = ld64(rs, so, 0);
                    }
                    const u32 nm = (sg.x & 0xffffu) ^ 0x8000u;
                    *(LAS u32x2*)(sm + L_ST + v * 256 + l31 * 8) = u32x2{nm | (nm << 16), __builtin_bit_cast(u32, __builtin_amdgcn_rcpf(bhi(sg.x)))};
                    *(LAS u32x2*)(sm + L_EP) = u32x2{e_lo, e_hi};
                    post(L_SR + v * 4, 1u);
                    SB();
                }
            }
        }
        STAMP(10);
        u32 wexp;
        {
            const u32 t_lo = e_lo << 8, t_hi = (e_hi << 8) | (e_lo >> 24);
            const u32 fo = WS_F1 + (b & 7) * 2048 + ta * 8;
            if (XDELAY) __builtin_amdgcn_s_sleep(XDELAY);
            u32x2 f;
#pragma nounroll
            for (u32 sp = 0; sp < SPIN_CAP; ++sp) {
                f = ld64(rs, fo, 0);
                if (all0(((f.x ^ t_lo) >> 8) | (f.y ^ t_hi))) break;
                __builtin_amdgcn_s_sleep(XSLEEP);
            }
            asm volatile("ds_max_u32 %0, %1" ::"v"(L_WEXP), "v"(f.x & 255u) : "memory");
            post(L_PA + v * 4, 2u); wait4(L_PA, 2u);
        }
        STAMP(11);
        {
            const int kd = b;
            const u32 xo = WS_X + i16 * 1024 + v * 256 + (g >> 1) * 64 + (g & 1) * 16, xb_ = ((kd & 7) | ((kd >> 4) << 3)) * 16384 + ((kd >> 3) & 1) * 32;
            const u32x4 x0 = ld128(rs, xo, xb_), x1 = ld128(rs, xo + 128, xb_);
            u32x4 wq[2], wn[2], wm[2], wk[2];
            wq[0] = ld128(rs, WS_W + ta * 32, b * 8192); wq[1] = ld128(rs, WS_W + ta * 32 + 16, b * 8192);
            if (b == 0) {
#pragma unroll
                for (int u = 0; u < 2; ++u) {
                    wn[u] = ld128(rs, WS_W + ta * 32 + u * 16, 256 * 8192);
                    wm[u] = ld128(rs, WS_W + ta * 32 + u * 16, 257 * 8192);
                    wk[u] = ld128(rs, WS_W + ta * 32 + u * 16, 258 * 8192);
                }
            }
            SB();
            wexp = __builtin_amdgcn_readfirstlane(*(LAS u32*)(sm + L_WEXP));
            wexp = wexp < 1u ? 1u : (wexp > 253u ? 253u : wexp);
            const float wsc = __builtin_bit_cast(float, (254u - wexp) << 23);
            {
                const u32 wa = L_SCR + (i16 >> 1) * 1040 + (g * 16 + 4 * v) * 16 + (i16 & 1) * 8;
#pragma unroll
                for (int e = 0; e < 4; ++e) {
                    const f32x4 t = mfma16h(e < 2 ? x0 : x1, (e & 1) ? f8a1 : f8a0, zero);
                    *(LAS u32x2*)(sm + wa + e * 16) = u32x2{pkh(t[0], t[1]), pkh(t[2], t[3])};
                }
            }
            post(L_PA + v * 4, 3u);
            STAMP(12);
            float pr[2][4], pi[2][4];
#pragma unroll
            for (int u = 0; u < 2; ++u)
#pragma unroll
                for (int r = 0; r < 4; ++r) { pr[u][r] = blo(wq[u][r]) * wsc; pi[u][r] = bhi(wq[u][r]) * wsc; PIN(pr[u][r]); PIN(pi[u][r]); }
            wait4(L_PA, 3u);
            STAMP(13);
            auto fwd = [&](int u, u32x4 d, f32x4& zr, f32x4& zi) {
                const f32x4 ur = mfma16h(d, c1r[u], zero), ui = mfma16h(d, c1i[u], zero);
                float a[4], c[4];
#pragma unroll
                for (int r = 0; r < 4; ++r) { a[r] = ur[r] * t1c[u][r] + ui[r] * t1s[u][r]; c[r] = ui[r] * t1c[u][r] - ur[r] * t1s[u][r]; }
                const u32x4 d2 = {pkh(a[0], a[1]), pkh(a[2], a[3]), pkh(c[0], c[1]), pkh(c[2], c[3])};
                zr = mfma16h(f16r, d2, zero); zi = mfma16h(f16i, d2, zero);
            };
            float yr[2][4], yi[2][4];
            {
                const u32x4 d0 = lds128(sm, L_SCR + (2 * v) * 1040 + lane * 16), d1 = lds128(sm, L_SCR + (2 * v + 1) * 1040 + lane * 16);
                f32x4 zr[2], zi[2];
                fwd(0, d0, zr[0], zi[0]); fwd(1, d1, zr[1], zi[1]);
                if (kd != 0) {
                    post(L_PA + v * 4, 4u);
#pragma unroll
                    for (int u = 0; u < 2; ++u)
#pragma unroll
                        for (int r = 0; r < 4; ++r) {
                            yr[u][r] = zr[u][r] * pr[u][r] - zi[u][r] * pi[u][r]; yi[u][r] = zr[u][r] * pi[u][r] + zi[u][r] * pr[u][r];
                        }
                } else {
#pragma unroll
                    for (int u = 0; u < 2; ++u) {
                        u32x4 d = lds128(sm, L_SCR + ((8 - 2 * v - u) & 7) * 1040 + lane * 16);
                        d.z ^= 0x80008000u; d.w ^= 0x80008000u;
                        f32x4 mr, mi; fwd(u, d, mr, mi);
#pragma unroll
                        for (int r = 0; r < 4; ++r) {
                            const float q4 = 0.25f * wsc;
                            const float h0r = 0.25f * pr[u][r] + q4 * blo(wm[u][r]), h0i = 0.25f * pi[u][r] - q4 * bhi(wm[u][r]);
                            const float h1r = q4 * blo(wn[u][r]) + q4 * blo(wk[u][r]), h1i = q4 * bhi(wn[u][r]) - q4 * bhi(wk[u][r]);
                            const float ar = h0r + h1r, ai = h0i + h1i, qr = h0r - h1r, qi = h0i - h1i;
                            yr[u][r] = zr[u][r] * ar - zi[u][r] * ai + mr[r] * qr - mi[r] * qi;
                            yi[u][r] = zr[u][r] * ai + zi[u][r] * ar + mr[r] * qi + mi[r] * qr;
                        }
                    }
                    post(L_PA + v * 4, 4u);
                }
            }
            f32x4 qre[2], qim[2];
#pragma unroll
            for (int u = 0; u < 2; ++u) {
                const u32x4 d3 = {pkh(yi[u][0], yi[u][1]), pkh(yi[u][2], yi[u][3]), pkh(yr[u][0], yr[u][1]), pkh(yr[u][2], yr[u][3])};
                const f32x4 vi = mfma16h(d3, f16r, zero), vr = mfma16h(d3, f16i, zero);
                float a[4], c[4];
#pragma unroll
                for (int r = 0; r < 4; ++r) { a[r] = vr[r] * t3c[u][r] - vi[r] * t3s[u][r]; c[r] = vi[r] * t3c[u][r] + vr[r] * t3s[u][r]; }
                const u32x4 d4 = {pkh(a[0], a[1]), pkh(a[2], a[3]), pkh(c[0], c[1]), pkh(c[2], c[3])};
                qre[u] = mfma16h(c4r[u], d4, zero); qim[u] = mfma16h(c4i[u], d4, zero);
            }
            wait4(L_PA, 4u);
#pragma unroll
            for (int r = 0; r < 4; ++r) {
                const u32 a = L_SCR + (16 * (4 * g + r) + i16) * 16 + v * 4;
                *(LAS u32*)(sm + a) = pkh(qre[0][r], qre[1][r]);
                *(LAS u32*)(sm + a + 4112) = pkh(qim[0][r], qim[1][r]);
            }
            post(L_PA + v * 4, 5u);
            STAMP(14);
            wait4(L_PA, 5u);
#pragma unroll
            for (int f = 0; f < 2; ++f) {
                const u32x4 d = lds128(sm, L_SCR + (g & 1) * 4112 + (64 * v + 32 * f + 2 * i16 + (g >> 1)) * 16);
                const f32x4 a = mfma16h(f8d0, d, zero), c = mfma16h(f8d1, d, zero);
                const u32x4 o = {pkh(a[0], a[1]), pkh(a[2], a[3]), pkh(c[0], c[1]), pkh(c[2], c[3])};
                st128(rs, o, WS_Y + (64 * v + 32 * f) * 32 + i16 * 64 + g * 16, kd * 8192);
            }
            STAMP(15);
        }
        {
            u32x4 gf[4][4];
            u32x4 xi0, xi1;
#pragma unroll
            for (int cc = 0; cc < 4; ++cc) {
                const int d = 64 * nt + 32 * (cc >> 1) + 8 * (i16 >> 2) + 4 * ((cc & 1) ^ ((i16 >> 2) & 1)) + (i16 & 3);
                const float step = (float)d * (1.f / 512.f);
#pragma unroll
                for (int jj = 0; jj < 4; ++jj) {
                    const int kd0 = 16 * (4 * jj + v) + 4 * g;
                    const float p0 = (float)((kd0 * d) & 511) * (1.f / 512.f);
#pragma unroll
                    for (int j = 0; j < 4; ++j) {
                        const float a = p0 + (float)j * step;
                        gf[jj][cc][j] = pkh(cosr(a), -sinr(a));
                    }
                }
                if (ta < 16) gf[0][cc][0] = (i16 & 1) ? 0xb8003800u : 0x38003800u;
#pragma unroll
                for (int jj = 0; jj < 4; ++jj) PIN(gf[jj][cc]);
                SB();
                if (cc == 1) {
                    WAITV(0);
                    if (arrive(L_CY) == 3) publish(rs, WS_FY, ((b >> 4) & 3) * 64 + (b >> 6) * 16 + (b & 15), 8, e_lo, e_hi, lane);
                    STAMP(16);
                    const u32 srow = 256 * (4 * (i16 & 1) + v) + 8 * T2 + (i16 >> 1);
                    const u32x4* xp = (const u32x4*)(p.x + srow * 512 + 64 * nt + 8 * g);
                    xi0 = xp[0]; xi1 = xp[4];
                    SB();
                }
            }
            STAMP(17);
            {
                const u32 fo = WS_FY + (b & 7) * 2048 + ta * 8;
                if (YDELAY) __builtin_amdgcn_s_sleep(YDELAY);
#pragma nounroll
                for (u32 sp = 0; sp < SPIN_CAP; ++sp) {
                    const u32x2 f = ld64(rs, fo, 0);
                    if (all0((f.x ^ e_lo) | (f.y ^ e_hi))) break;
                    __builtin_amdgcn_s_sleep(YSLEEP);
                }
                asm volatile("" ::: "memory");
            }
            STAMP(18);
            u32x4 yl[4][4];
#pragma unroll
            for (int jj = 0; jj < 4; ++jj)
#pragma unroll
                for (int j = 0; j < 4; ++j) yl[jj][j] = ld128(rs, WS_Y + 4 * g * 8192 + i16 * 16, (u32)(16 * (4 * jj + v) + j) * 8192 + T2 * 256);
            SB();
            f32x4 acc[4][4];
#pragma unroll
            for (int c = 0; c < 4; ++c)
#pragma unroll
                for (int cc = 0; cc < 4; ++cc) acc[c][cc] = f32x4{0.f, 0.f, 0.f, 0.f};
#pragma unroll
            for (int jj = 0; jj < 4; ++jj)
#pragma unroll
                for (int c = 0; c < 4; ++c) {
                    const u32x4 yf = {yl[jj][0][c], yl[jj][1][c], yl[jj][2][c], yl[jj][3][c]};
#pragma unroll
                    for (int cc = 0; cc < 4; ++cc) acc[c][cc] = mfma16h(gf[jj][cc], yf, acc[c][cc]);
                }
            STAMP(19);
            wait4(L_FREE, 1u);
            STAMP(20);
#pragma unroll
            for (int k = 1; k < 4; ++k)
#pragma unroll
                for (int cc = 0; cc < 4; ++cc) *(LAS f32x4*)(sm + L_XS + (v * 3 + k - 1) * 4096 + cc * 1024 + lane * 16) = acc[k][cc];
            post(L_PA + v * 4, 6u); wait4(L_PA, 6u);
            if (b == 0 && ta == 0) { u32x2 e = {e_lo, e_hi}; st64(rs, e, WS_EPOCH, 0); }
            f32x4 o[4];
#pragma unroll
            for (int cc = 0; cc < 4; ++cc) o[cc] = acc[0][cc];
#pragma unroll
            for (int k = 1; k < 4; ++k) {
                const int src = (v - k) & 3;
#pragma unroll
                for (int cc = 0; cc < 4; ++cc) o[cc] += ldsf4(sm, L_XS + (src * 3 + k - 1) * 4096 + cc * 1024 + lane * 16);
            }
            const float alpha = __builtin_bit_cast(float, wexp << 23) * 0.125f;
            const bool odd = g & 1;
            u32x4 z0, z1;
#pragma unroll
            for (int cc = 0; cc < 4; ++cc)
#pragma unroll
                for (int e = 0; e < 2; ++e) {
                    const u32x4 xx = cc < 2 ? xi0 : xi1;
                    const u32 xv = odd ? xx[2 * ((cc & 1) ^ 1) + e] : xx[2 * (cc & 1) + e];
                    const u32 y = pkb(o[cc][2 * e] * alpha, o[cc][2 * e + 1] * alpha);
                    const u32 z = pkb(blo(xv) + blo(y), bhi(xv) + bhi(y));
                    if (cc < 2) z0[2 * cc + e] = z; else z1[2 * cc - 4 + e] = z;
                }
            *(LAS u32x4*)(sm + L_SCR + v * 2048 + lane * 16) = z0;
            *(LAS u32x4*)(sm + L_SCR + v * 2048 + 1024 + lane * 16) = z1;
            post(L_ZR + v * 4, 1u);
            STAMP(21);
            STAMP_OUT();
        }
    }
}

#if defined(__HIP_DEVICE_COMPILE__)
#pragma clang attribute pop
#endif

extern "C" void kernel_launch(void* const* d_in, const int* in_sizes, int n_in,
                              void* d_out, int out_size, void* d_ws, size_t ws_size,
                              hipStream_t stream) {
    Params p;
    p.x = (const u16*)d_in[0]; p.wr = (const u16*)d_in[1]; p.wi = (const u16*)d_in[2];
    p.w1 = (const u16*)d_in[4]; p.w2 = (const u16*)d_in[5];
    p.out = (u16*)d_out; p.ws = (unsigned char*)d_ws;
    fused_kernel<<<256, 512, 0, stream>>>(p);
}
```
